```python
import torch

def _softmax(x, axis=-1):
    return torch.softmax(x, dim=axis)
def _gelu(x):
    return torch.nn.functional.gelu(x, approximate="tanh")
def _silu(x):
    return torch.nn.functional.silu(x)
def _sigmoid(x):
    return torch.sigmoid(x)
def _layernorm(x, axis=-1, eps=1e-5):
    m = x.mean(dim=axis, keepdim=True)
    v = x.var(dim=axis, keepdim=True, unbiased=False)
    return (x - m) / torch.sqrt(v + eps)
def _scatter_add(tgt, idx, src, axis=0):
    return tgt.clone().index_add_(axis, idx, src)
def _segment_sum(src, seg, n):
    out = torch.zeros((n,) + src.shape[1:], dtype=src.dtype)
    return out.index_add_(0, seg, src)
def _pad(x, pw):
    flat = tuple(v for pair in reversed(pw) for v in pair)
    return torch.nn.functional.pad(x, flat)

kernel_name = 'standalone__mha_softmax__near_ridge'

def setup_inputs():

    _w = (lambda a: (lambda t: t.to(torch.bfloat16) if t.is_floating_point() else t)(torch.as_tensor(a)))
    B, H, Sq, Sk, D = 8, 32, 384, 384, 128
    return {
        "q": _w((lambda *s: torch.randn(*s, dtype=torch.bfloat16) * 0.8)(B, H, Sq, D)),
        "k": _w((lambda *s: torch.randn(*s, dtype=torch.bfloat16) * 0.8)(B, H, Sk, D)),
        "v": _w((lambda *s: torch.randn(*s, dtype=torch.bfloat16) * 0.05)(B, H, Sk, D)),
    }

def reference(q, k, v):
    scale = q.shape[-1] ** -0.5
    scores = torch.einsum("bhqd,bhkd->bhqk", q, k) * scale
    p = _softmax(scores, axis=-1)
    return torch.einsum("bhqk,bhkd->bhqd", p, v)
```

```cpp
#include <hip/hip_runtime.h>
#include <hip/hip_bf16.h>
using bf16 = __hip_bfloat16;
constexpr int D = 128, NW = 4, QBLK = 32, KVBLK = 64;
constexpr float SCALE = 0.088388347648318440f;
constexpr float THR = 8.f;
constexpr int BH = 256, S = 384;
constexpr size_t SHM_V = KVBLK * D * 2, SHM_K = KVBLK * D * 2, SHM_ATTN = 2 * SHM_V + 2 * SHM_K + NW * 64 * 4;
using bf16x8 = __attribute__((ext_vector_type(8))) short;
using s16x4  = __attribute__((ext_vector_type(4))) short;
using f32x16 = __attribute__((ext_vector_type(16))) float;
using u32x4  = __attribute__((ext_vector_type(4))) unsigned;
#define KSWZ(row, colB) ((row) * 256 + ((colB) ^ (((row) & 7) << 4)))
#define SBAR() __builtin_amdgcn_sched_barrier(0)
__device__ __forceinline__ int crow(int r, int hi) { return (r & 3) + 8 * (r >> 2) + 4 * hi; }
__device__ __forceinline__ unsigned cvtpk(float lo, float hi) {
  unsigned r; asm volatile("v_cvt_pk_bf16_f32 %0, %1, %2" : "=v"(r) : "v"(lo), "v"(hi)); return r;
}
__device__ __forceinline__ void partialSM(f32x16& p0, f32x16& p1, float& m_reg, float& mn, float& alpha) {
  constexpr float C = SCALE * 1.4426950408889634f;
  float pmax = p0[0]; for (int r = 1; r < 16; ++r) pmax = fmaxf(pmax, p0[r]); for (int r = 0; r < 16; ++r) pmax = fmaxf(pmax, p1[r]);
  { auto rr = __builtin_amdgcn_permlane32_swap(__float_as_uint(pmax), __float_as_uint(pmax), false, false);
    pmax = fmaxf(__uint_as_float(rr[0]), __uint_as_float(rr[1])); }
  if (__builtin_expect(__all(pmax - m_reg <= THR / SCALE), 1)) { mn = m_reg; alpha = 1.f; }
  else { mn = fmaxf(m_reg, pmax); alpha = __builtin_amdgcn_exp2f((m_reg - mn) * C); m_reg = mn; }
  float mnC = -mn * C;
  for (int r = 0; r < 16; ++r) p0[r] = fmaf(p0[r], C, mnC); for (int r = 0; r < 16; ++r) p1[r] = fmaf(p1[r], C, mnC);
  for (int r = 0; r < 16; ++r) p0[r] = __builtin_amdgcn_exp2f(p0[r]);
}
__device__ __forceinline__ void finishSM(f32x16& p0, f32x16& p1, float alpha, float& l_reg, bf16x8& pa0, bf16x8& pa1, bf16x8& pa2, bf16x8& pa3) {
  for (int r = 0; r < 16; ++r) p1[r] = __builtin_amdgcn_exp2f(p1[r]);
  float ps = 0; for (int r = 0; r < 16; ++r) ps += p0[r]; for (int r = 0; r < 16; ++r) ps += p1[r];
  { auto rr = __builtin_amdgcn_permlane32_swap(__float_as_uint(ps), __float_as_uint(ps), false, false);
    ps = __uint_as_float(rr[0]) + __uint_as_float(rr[1]); }
  l_reg = l_reg * alpha + ps;
#define PK4(P, BASE, OUT) do { unsigned a0 = cvtpk(P[BASE + 0], P[BASE + 1]), a1 = cvtpk(P[BASE + 2], P[BASE + 3]);   \
    unsigned b0 = cvtpk(P[BASE + 4], P[BASE + 5]), b1 = cvtpk(P[BASE + 6], P[BASE + 7]);                              \
    auto r0 = __builtin_amdgcn_permlane32_swap(a0, b0, false, false); auto r1 = __builtin_amdgcn_permlane32_swap(a1, b1, false, false); \
    u32x4 w = {r0[0], r1[0], r0[1], r1[1]}; OUT = *reinterpret_cast<bf16x8*>(&w); } while (0)
  PK4(p0, 0, pa0); PK4(p0, 8, pa1); PK4(p1, 0, pa2); PK4(p1, 8, pa3);
#undef PK4
}
__device__ __forceinline__ void qkt(f32x16& p0, f32x16& p1, const bf16* Ks, const bf16x8* qr, int r32, int hi) {
  p0 = f32x16{}; p1 = f32x16{};
  for (int d0 = 0; d0 < 8; ++d0) { int cb = (d0 * 16 + hi * 8) * 2;
    bf16x8 b0 = *reinterpret_cast<const bf16x8*>((const char*)Ks + KSWZ(r32, cb));
    bf16x8 b1 = *reinterpret_cast<const bf16x8*>((const char*)Ks + KSWZ(32 + r32, cb));
    p0 = __builtin_amdgcn_mfma_f32_32x32x16_bf16(b0, qr[d0], p0, 0, 0, 0);
    p1 = __builtin_amdgcn_mfma_f32_32x32x16_bf16(b1, qr[d0], p1, 0, 0, 0); }
}
__device__ __forceinline__ int v_st(int k, int c) { const int kk = (k & ~0xC) | ((k & 4) << 1) | ((k & 8) >> 1); return ((kk >> 3) * 4 + (c >> 5)) * 512 + ((kk & 7) * 32 + (c & 31)) * 2; }
__device__ __forceinline__ int v_rd_base(int lane) { return ((lane & 3) << 3) | (((lane >> 2) & 3) << 6) | (((lane >> 4) & 1) << 5) | (((lane >> 5) & 1) << 8); }
constexpr int v_rd_off(int d0, int ks, int half) { return d0 * 512 + ks * 4096 + half * 2048; }
template <int OFF> __device__ __forceinline__ s16x4 tr_read(int vb) {
  s16x4 r; asm volatile("ds_read_b64_tr_b16 %0, %1 offset:%2" : "=&v"(r) : "v"(vb), "i"(OFF) : "memory"); return r;
}
template <int D0> __device__ __forceinline__ void pv_one(f32x16& od, int vb, bf16x8 pa0, bf16x8 pa1, bf16x8 pa2, bf16x8 pa3) {
  const s16x4 l0 = tr_read<v_rd_off(D0, 0, 0)>(vb), h0 = tr_read<v_rd_off(D0, 0, 1)>(vb), l1 = tr_read<v_rd_off(D0, 1, 0)>(vb), h1 = tr_read<v_rd_off(D0, 1, 1)>(vb);
  const s16x4 l2 = tr_read<v_rd_off(D0, 2, 0)>(vb), h2 = tr_read<v_rd_off(D0, 2, 1)>(vb), l3 = tr_read<v_rd_off(D0, 3, 0)>(vb), h3 = tr_read<v_rd_off(D0, 3, 1)>(vb);
  asm volatile("s_waitcnt lgkmcnt(0)" ::: "memory"); SBAR();
#define PK(L, H) (bf16x8){L[0], L[1], L[2], L[3], H[0], H[1], H[2], H[3]}
  od = __builtin_amdgcn_mfma_f32_32x32x16_bf16(pa0, PK(l0, h0), od, 0, 0, 0);
  od = __builtin_amdgcn_mfma_f32_32x32x16_bf16(pa1, PK(l1, h1), od, 0, 0, 0);
  od = __builtin_amdgcn_mfma_f32_32x32x16_bf16(pa2, PK(l2, h2), od, 0, 0, 0);
  od = __builtin_amdgcn_mfma_f32_32x32x16_bf16(pa3, PK(l3, h3), od, 0, 0, 0);
#undef PK
}
__device__ __forceinline__ void pv_d0(f32x16* o, int vb, bf16x8 pa0, bf16x8 pa1, bf16x8 pa2, bf16x8 pa3) {
  pv_one<0>(o[0], vb, pa0, pa1, pa2, pa3); pv_one<1>(o[1], vb, pa0, pa1, pa2, pa3); pv_one<2>(o[2], vb, pa0, pa1, pa2, pa3); pv_one<3>(o[3], vb, pa0, pa1, pa2, pa3);
}
__global__ __launch_bounds__(64 * NW, 2)
void attn_k(const bf16* __restrict__ Q, const bf16* __restrict__ K, const bf16* __restrict__ V, bf16* __restrict__ O) {
  extern __shared__ __attribute__((aligned(16))) char lds[];
  const int bid = blockIdx.x;
  const int head = (bid / 24) * 8 + (bid % 8), qb = (bid % 24) / 8;
  const bf16* Qb = Q + ((long)head * S + qb * 128) * D; const bf16* Kh = K + (long)head * S * D; const bf16* Vh = V + (long)head * S * D;
  bf16* Ob = O + ((long)head * S + qb * 128) * D;
  const int seq = S;
  const int tid = threadIdx.x, wid = tid >> 6, lane = tid & 63, r32 = lane & 31, hi = lane >> 5;
  bf16* V_lds = (bf16*)lds; bf16* K_lds = (bf16*)(lds + 2 * SHM_V);
  float* ws = (float*)(lds + 2 * SHM_V + 2 * SHM_K) + wid * 64; float* li_l = ws; float* al_l = ws + 32;
  float m_reg = -1e30f, l_reg = 0; f32x16 o[4] = {}; bf16x8 qr[8];
  const bf16* Qw = Qb + (long)(wid * QBLK + r32) * D + hi * 8;
#pragma unroll
  for (int d0 = 0; d0 < 8; ++d0) qr[d0] = *reinterpret_cast<const bf16x8*>(Qw + d0 * 16);
  const int sr = tid >> 4, sc = (tid & 15) * 8;
  const int vb0 = (int)(uintptr_t)V_lds + v_rd_base(lane);
  bf16x8 sv[4], sk[4];
#define SLOAD(k0) do { _Pragma("unroll") for (int i = 0; i < 4; ++i) { sv[i] = *reinterpret_cast<const bf16x8*>(&Vh[(long)((k0) + 16 * i + sr) * D + sc]); sk[i] = *reinterpret_cast<const bf16x8*>(&Kh[(long)((k0) + 16 * i + sr) * D + sc]); } } while (0)
#define SWRITE(b) do { _Pragma("unroll") for (int i = 0; i < 4; ++i) { *(bf16x8*)((char*)V_lds + (b) * SHM_V + v_st(16 * i + sr, sc)) = sv[i]; *(bf16x8*)((char*)K_lds + (b) * SHM_K + KSWZ(16 * i + sr, sc * 2)) = sk[i]; } } while (0)
#define RESC(a) do { if (__any((a) < 1.f)) { if (hi == 0) al_l[r32] = (a); asm volatile("s_waitcnt lgkmcnt(0)" ::: "memory"); \
    for (int d = 0; d < 4; ++d) for (int r = 0; r < 16; ++r) o[d][r] *= al_l[crow(r, hi)]; } } while (0)
  f32x16 pA0, pA1, pB0, pB1; float mnA, mnB, alA, alB; bf16x8 pa0, pa1, pa2, pa3; const int NT = seq / KVBLK;
  SLOAD(0); SWRITE(0); __syncthreads();
  qkt(pA0, pA1, K_lds, qr, r32, hi); partialSM(pA0, pA1, m_reg, mnA, alA);
  SLOAD(KVBLK); SWRITE(1); __syncthreads();
  for (int j = 1; j + 1 < NT; j += 2) {
    SBAR(); qkt(pB0, pB1, (bf16*)((char*)K_lds + SHM_K), qr, r32, hi);
    finishSM(pA0, pA1, alA, l_reg, pa0, pa1, pa2, pa3); SBAR();
    SLOAD((j + 1) * KVBLK); SBAR();
    pv_d0(o, vb0, pa0, pa1, pa2, pa3); partialSM(pB0, pB1, m_reg, mnB, alB);
    __syncthreads(); SWRITE(0);
    RESC(alB); __syncthreads();
    SBAR(); qkt(pA0, pA1, K_lds, qr, r32, hi);
    finishSM(pB0, pB1, alB, l_reg, pa0, pa1, pa2, pa3); SBAR();
    SLOAD((j + 2) * KVBLK); SBAR();
    pv_d0(o, vb0 + (int)SHM_V, pa0, pa1, pa2, pa3); partialSM(pA0, pA1, m_reg, mnA, alA);
    __syncthreads(); SWRITE(1);
    RESC(alA); __syncthreads();
  }
  SBAR(); qkt(pB0, pB1, (bf16*)((char*)K_lds + SHM_K), qr, r32, hi);
  finishSM(pA0, pA1, alA, l_reg, pa0, pa1, pa2, pa3); SBAR();
  pv_d0(o, vb0, pa0, pa1, pa2, pa3); partialSM(pB0, pB1, m_reg, mnB, alB);
  __syncthreads(); RESC(alB);
  finishSM(pB0, pB1, alB, l_reg, pa0, pa1, pa2, pa3); SBAR();
  pv_d0(o, vb0 + (int)SHM_V, pa0, pa1, pa2, pa3);
  if (hi == 0) li_l[r32] = l_reg; asm volatile("s_waitcnt lgkmcnt(0)" ::: "memory");
  float rli[16];
#pragma unroll
  for (int r = 0; r < 16; ++r) rli[r] = __builtin_amdgcn_rcpf(li_l[crow(r, hi)]);
  bf16* Ow = Ob + (long)(wid * QBLK) * D;
#pragma unroll
  for (int r = 0; r < 16; ++r) { int orow = crow(r, hi);
    for (int d0 = 0; d0 < 4; ++d0) Ow[(long)orow * D + d0 * 32 + r32] = __float2bfloat16(o[d0][r] * rli[r]); }
}
extern "C" void kernel_launch(void* const* d_in, const int* in_sizes, int n_in, void* d_out, int out_size, void* d_ws, size_t ws_size, hipStream_t stream) {
  (void)hipFuncSetAttribute((const void*)attn_k, hipFuncAttributeMaxDynamicSharedMemorySize, (int)SHM_ATTN);
  hipLaunchKernelGGL(attn_k, dim3(768), dim3(64 * NW), SHM_ATTN, stream, (const bf16*)d_in[0], (const bf16*)d_in[1], (const bf16*)d_in[2], (bf16*)d_out);
}
```

```cpp
#include <hip/hip_runtime.h>
#include <hip/hip_bf16.h>

namespace {
constexpr int S_LEN = 384, HD = 128;
typedef unsigned short u16;
__device__ __forceinline__ float bf2f(u16 x) { return __uint_as_float((unsigned)x << 16); }

__device__ __forceinline__ void fallback_rows(const u16* __restrict__ Q, const u16* __restrict__ K, const u16* __restrict__ V,
                                           __hip_bfloat16* __restrict__ O, int row0, int lane) {
  for (int r = row0; r < row0 + 96; ++r) {
    const u16* q = Q + r * HD;
    float s0, s1, s2, s3, s4, s5;
    s0 = s1 = s2 = s3 = s4 = s5 = 0.f;
    for (int d = 0; d < HD; ++d) {
      const float qv = bf2f(q[d]);
      s0 = fmaf(qv, bf2f(K[(lane) * HD + d]), s0);
      s1 = fmaf(qv, bf2f(K[(lane + 64) * HD + d]), s1);
      s2 = fmaf(qv, bf2f(K[(lane + 128) * HD + d]), s2);
      s3 = fmaf(qv, bf2f(K[(lane + 192) * HD + d]), s3);
      s4 = fmaf(qv, bf2f(K[(lane + 256) * HD + d]), s4);
      s5 = fmaf(qv, bf2f(K[(lane + 320) * HD + d]), s5);
    }
    float m = fmaxf(fmaxf(fmaxf(s0, s1), fmaxf(s2, s3)), fmaxf(s4, s5));
    for (int o = 32; o > 0; o >>= 1) m = fmaxf(m, __shfl_xor(m, o));
    const float c = 0.12751743082f;
    s0 = exp2f((s0 - m) * c); s1 = exp2f((s1 - m) * c); s2 = exp2f((s2 - m) * c);
    s3 = exp2f((s3 - m) * c); s4 = exp2f((s4 - m) * c); s5 = exp2f((s5 - m) * c);
    float l = ((s0 + s1) + (s2 + s3)) + (s4 + s5);
    for (int o = 32; o > 0; o >>= 1) l += __shfl_xor(l, o);
    float o0 = 0.f, o1 = 0.f;
    for (int k = 0; k < 64; ++k) {
      const float p0 = __shfl(s0, k), p1 = __shfl(s1, k), p2 = __shfl(s2, k), p3 = __shfl(s3, k), p4 = __shfl(s4, k), p5 = __shfl(s5, k);
      const u16* v = V + k * HD + 2 * lane;
      o0 = fmaf(p0, bf2f(v[0]), o0);            o1 = fmaf(p0, bf2f(v[1]), o1);
      o0 = fmaf(p1, bf2f(v[64 * HD]), o0);      o1 = fmaf(p1, bf2f(v[64 * HD + 1]), o1);
      o0 = fmaf(p2, bf2f(v[128 * HD]), o0);     o1 = fmaf(p2, bf2f(v[128 * HD + 1]), o1);
      o0 = fmaf(p3, bf2f(v[192 * HD]), o0);     o1 = fmaf(p3, bf2f(v[192 * HD + 1]), o1);
      o0 = fmaf(p4, bf2f(v[256 * HD]), o0);     o1 = fmaf(p4, bf2f(v[256 * HD + 1]), o1);
      o0 = fmaf(p5, bf2f(v[320 * HD]), o0);     o1 = fmaf(p5, bf2f(v[320 * HD + 1]), o1);
    }
    const float il = 1.0f / l;
    O[r * HD + 2 * lane] = __float2bfloat16(o0 * il);
    O[r * HD + 2 * lane + 1] = __float2bfloat16(o1 * il);
  }
}
}

__global__ __launch_bounds__(256, 1) void mha_fwd_kernel(const u16* __restrict__ Q, const u16* __restrict__ K, const u16* __restrict__ V,
                                                         __hip_bfloat16* __restrict__ O) {
  __shared__ __attribute__((aligned(1024))) char lds[163840];
  const unsigned head = blockIdx.x, tid = threadIdx.x;
  const unsigned long long ka = (unsigned long long)__builtin_amdgcn_kernarg_segment_ptr();
  const unsigned ldsb = (unsigned)(size_t)lds;
  unsigned long long flag;
  asm volatile(
      "s_load_dwordx2 s[16:17], %1, 0\n"
      "s_load_dwordx2 s[20:21], %1, 8\n"
      "s_load_dwordx2 s[24:25], %1, 16\n"
      "s_load_dwordx2 s[28:29], %1, 24\n"
      "s_getpc_b64 s[48:49]\n"
      "s_lshr_b32 s39, %2, 3\n"
      "s_lshl_b32 s39, s39, 3\n"
      "v_and_b32_e32 v72, 15, %3\n"
      "v_add_u32_e32 v72, s39, v72\n"
      "v_and_b32_e32 v72, 0xff, v72\n"
      "v_lshlrev_b32_e32 v72, 7, v72\n"
      "v_min_u32_e32 v72, 0x7580, v72\n"
      "global_load_dword v95, v72, s[48:49]\n"
      "v_lshrrev_b32_e32 v19, 6, %3\n"
      "v_and_b32_e32 v23, 63, %3\n"
      "s_mul_i32 s42, %2, 98304\n"
      "v_readfirstlane_b32 s36, v19\n"
      "v_lshrrev_b32_e32 v19, 3, v23\n"
      "v_lshrrev_b32_e32 v20, 4, v23\n"
      "v_and_b32_e32 v21, 7, v23\n"
      "v_lshlrev_b32_e32 v19, 8, v19\n"
      "v_xor_b32_e32 v9, v20, v21\n"
      "v_lshl_add_u32 v9, v9, 4, v19\n"
      "s_lshl_b32 s43, s36, 11\n"
      "s_add_u32 s40, s43, %4\n"
      "s_and_b32 s47, s36, 1\n"
      "s_lshl_b32 s47, s47, 6\n"
      "v_xor_b32_e32 v7, s47, v9\n"
      "v_add_u32_e32 v7, s43, v7\n"
      "v_lshlrev_b32_e32 v20, 1, v20\n"
      "v_xor_b32_e32 v8, v20, v21\n"
      "v_lshl_add_u32 v8, v8, 4, v19\n"
      "v_add_u32_e32 v8, s43, v8\n"
      "s_mul_i32 s43, s36, 24576\n"
      "s_lshl_b32 s47, s36, 13\n"
      "s_add_u32 s41, s47, %4\n"
      "s_lshl_b32 s51, s36, 12\n"
      "s_add_u32 s50, s51, %4\n"
      "v_xor_b32_e32 v10, 64, v9\n"
      "v_add_u32_e32 v9, s43, v9\n"
      "v_add_u32_e32 v10, s43, v10\n"
      "v_add_u32_e32 v10, 0x800, v10\n"
      "v_and_b32_e32 v19, 15, v23\n"
      "v_lshrrev_b32_e32 v20, 4, v23\n"
      "v_lshlrev_b32_e32 v88, 4, v23\n"
      "v_add_u32_e32 v88, s43, v88\n"
      "v_lshrrev_b32_e32 v21, 3, v19\n"
      "v_lshlrev_b32_e32 v21, 11, v21\n"
      "v_and_b32_e32 v22, 7, v19\n"
      "v_lshl_add_u32 v21, v22, 7, v21\n"
      "v_bfe_u32 v22, v19, 1, 3\n"
      "v_xor_b32_e32 v22, v22, v20\n"
      "v_lshl_add_u32 v1, v22, 4, v21\n"
      "v_add_u32_e32 v1, %4, v1\n"
      "v_xor_b32_e32 v2, 64, v1\n"
      "s_add_u32 s43, s47, 131072\n"
      "v_add_u32_e32 v11, s43, v1\n"
      "v_add_u32_e32 v12, s43, v2\n"
      "v_mul_u32_u24_e32 v21, 0x110, v19\n"
      "v_lshl_add_u32 v21, v20, 3, v21\n"
      "v_add_u32_e32 v89, s43, v21\n"
      "v_add_u32_e32 v89, %4, v89\n"
      "v_mul_u32_u24_e32 v21, 0x110, v20\n"
      "v_lshl_add_u32 v21, v19, 4, v21\n"
      "v_add_u32_e32 v90, s43, v21\n"
      "v_add_u32_e32 v90, %4, v90\n"
      "v_bfe_u32 v19, v23, 2, 2\n"
      "v_and_b32_e32 v22, 1, v20\n"
      "v_lshl_add_u32 v21, v22, 2, v19\n"
      "v_lshlrev_b32_e32 v21, 7, v21\n"
      "v_lshrrev_b32_e32 v20, 1, v20\n"
      "v_lshl_add_u32 v21, v20, 11, v21\n"
      "v_lshrrev_b32_e32 v19, 1, v19\n"
      "v_lshl_add_u32 v19, v22, 1, v19\n"
      "v_lshl_add_u32 v21, v19, 5, v21\n"
      "v_and_b32_e32 v19, 3, v23\n"
      "v_lshl_add_u32 v21, v19, 3, v21\n"
      "v_add_u32_e32 v21, 65536, v21\n"
      "v_add_u32_e32 v3, %4, v21\n"
      "v_xor_b32_e32 v4, 0x20, v3\n"
      "v_xor_b32_e32 v5, 0x40, v3\n"
      "v_xor_b32_e32 v6, 0x60, v3\n"
      "v_mov_b32_e32 v13, 0\n"
      "v_mov_b32_e32 v14, 0\n"
      "v_mov_b32_e32 v15, 0\n"
      "v_mov_b32_e32 v16, 0\n"
      "v_mov_b32_e32 v17, 0\n"
      "v_mov_b32_e32 v18, 0\n"
      "s_mov_b32 s37, 0x3e0293ee\n"
      "s_mov_b32 s52, s37\n"
      "s_mov_b32 s53, s37\n"
      "s_mov_b32 s46, 0x63800000\n"
      "s_mov_b64 s[44:45], 0\n"
      "s_mov_b32 s22, 98304\n"
      "s_mov_b32 s23, 0x00020000\n"
      "s_mov_b32 s18, 98304\n"
      "s_mov_b32 s19, 0x00020000\n"
      "s_mov_b32 s26, 98304\n"
      "s_mov_b32 s27, 0x00020000\n"
      "s_mov_b32 s30, 98304\n"
      "s_mov_b32 s31, 0x00020000\n"
      "s_waitcnt lgkmcnt(0)\n"
      "s_add_u32 s20, s20, s42\n"
      "s_addc_u32 s21, s21, 0\n"
      "s_and_b32 s21, s21, 0xffff\n"
      "s_add_u32 m0, s40, 0\n"
      "s_mov_b32 s38, 0\n"
      "buffer_load_dwordx4 v7, s[20:23], s38 offen nt lds\n"
      "s_add_u32 m0, s40, 896\n"
      "s_nop 0\n"
      "buffer_load_dwordx4 v7, s[20:23], s38 offen offset:128 nt lds\n"
      "s_add_u32 s16, s16, s42\n"
      "s_addc_u32 s17, s17, 0\n"
      "s_and_b32 s17, s17, 0xffff\n"
      "s_add_u32 m0, s41, 131072\n"
      "s_nop 0\n"
      "buffer_load_dwordx4 v9, s[16:19], s38 offen nt lds\n"
      "s_add_u32 m0, s41, 131968\n"
      "s_nop 0\n"
      "buffer_load_dwordx4 v9, s[16:19], s38 offen offset:128 nt lds\n"
      "s_add_u32 m0, s41, 133120\n"
      "s_nop 0\n"
      "buffer_load_dwordx4 v10, s[16:19], s38 offen nt lds\n"
      "s_add_u32 m0, s41, 134016\n"
      "s_nop 0\n"
      "buffer_load_dwordx4 v10, s[16:19], s38 offen offset:128 nt lds\n"
      "s_add_u32 s24, s24, s42\n"
      "s_addc_u32 s25, s25, 0\n"
      "s_and_b32 s25, s25, 0xffff\n"
      "s_add_u32 m0, s40, 65536\n"
      "s_nop 0\n"
      "buffer_load_dwordx4 v8, s[24:27], s38 offen nt lds\n"
      "s_add_u32 m0, s40, 66432\n"
      "s_nop 0\n"
      "buffer_load_dwordx4 v8, s[24:27], s38 offen offset:128 nt lds\n"
      "s_add_u32 s28, s28, s42\n"
      "s_addc_u32 s29, s29, 0\n"
      "s_and_b32 s29, s29, 0xffff\n"
      "s_add_u32 m0, s41, 135168\n"
      "s_mov_b32 s38, 4096\n"
      "buffer_load_dwordx4 v9, s[16:19], s38 offen nt lds\n"
      "s_add_u32 m0, s41, 136064\n"
      "s_nop 0\n"
      "buffer_load_dwordx4 v9, s[16:19], s38 offen offset:128 nt lds\n"
      "s_add_u32 m0, s41, 137216\n"
      "s_nop 0\n"
      "buffer_load_dwordx4 v10, s[16:19], s38 offen nt lds\n"
      "s_add_u32 m0, s41, 138112\n"
      "s_nop 0\n"
      "buffer_load_dwordx4 v10, s[16:19], s38 offen offset:128 nt lds\n"
      "s_waitcnt vmcnt(10)\n"
      "s_barrier\n"
      "ds_read_b128 a[96:99], v1 offset:0\n"
      "ds_read_b128 a[100:103], v2 offset:0\n"
      "ds_read_b128 a[104:107], v1 offset:1024\n"
      "ds_read_b128 a[108:111], v2 offset:1024\n"
      "ds_read_b128 a[112:115], v1 offset:4096\n"
      "ds_read_b128 a[116:119], v2 offset:4096\n"
      "ds_read_b128 a[120:123], v1 offset:5120\n"
      "ds_read_b128 a[124:127], v2 offset:5120\n"
      "s_waitcnt vmcnt(6)\n"
      "ds_read_b128 v[72:75], v11 offset:0\n"
      "ds_read_b128 v[76:79], v12 offset:0\n"
      "ds_read_b128 v[80:83], v11 offset:1024\n"
      "ds_read_b128 v[84:87], v12 offset:1024\n"
      "s_waitcnt lgkmcnt(3)\n"
      "v_lshlrev_b32_e32 v20, 16, v72\n"
      "v_and_b32_e32 v21, 0xffff0000, v72\n"
      "v_pk_mul_f32 v[20:21], v[20:21], s[52:53]\n"
      "v_lshlrev_b32_e32 v22, 16, v73\n"
      "v_and_b32_e32 v23, 0xffff0000, v73\n"
      "v_cvt_pk_bf16_f32 v72, v20, v21\n"
      "v_pk_mul_f32 v[22:23], v[22:23], s[52:53]\n"
      "v_lshlrev_b32_e32 v92, 16, v74\n"
      "v_and_b32_e32 v93, 0xffff0000, v74\n"
      "v_accvgpr_write_b32 a0, v72\n"
      "v_cvt_pk_bf16_f32 v73, v22, v23\n"
      "v_pk_mul_f32 v[92:93], v[92:93], s[52:53]\n"
      "v_lshlrev_b32_e32 v20, 16, v75\n"
      "v_and_b32_e32 v21, 0xffff0000, v75\n"
      "v_accvgpr_write_b32 a1, v73\n"
      "v_cvt_pk_bf16_f32 v74, v92, v93\n"
      "v_pk_mul_f32 v[20:21], v[20:21], s[52:53]\n"
      "s_waitcnt lgkmcnt(2)\n"
      "v_lshlrev_b32_e32 v22, 16, v76\n"
      "v_and_b32_e32 v23, 0xffff0000, v76\n"
      "v_accvgpr_write_b32 a2, v74\n"
      "v_cvt_pk_bf16_f32 v75, v20, v21\n"
      "v_pk_mul_f32 v[22:23], v[22:23], s[52:53]\n"
      "v_lshlrev_b32_e32 v92, 16, v77\n"
      "v_and_b32_e32 v93, 0xffff0000, v77\n"
      "v_accvgpr_write_b32 a3, v75\n"
      "v_cvt_pk_bf16_f32 v76, v22, v23\n"
      "v_pk_mul_f32 v[92:93], v[92:93], s[52:53]\n"
      "v_lshlrev_b32_e32 v20, 16, v78\n"
      "v_and_b32_e32 v21, 0xffff0000, v78\n"
      "v_accvgpr_write_b32 a4, v76\n"
      "v_cvt_pk_bf16_f32 v77, v92, v93\n"
      "v_pk_mul_f32 v[20:21], v[20:21], s[52:53]\n"
      "v_lshlrev_b32_e32 v22, 16, v79\n"
      "v_and_b32_e32 v23, 0xffff0000, v79\n"
      "v_accvgpr_write_b32 a5, v77\n"
      "v_cvt_pk_bf16_f32 v78, v20, v21\n"
      "v_pk_mul_f32 v[22:23], v[22:23], s[52:53]\n"
      "s_waitcnt lgkmcnt(1)\n"
      "v_lshlrev_b32_e32 v92, 16, v80\n"
      "v_and_b32_e32 v93, 0xffff0000, v80\n"
      "v_accvgpr_write_b32 a6, v78\n"
      "v_cvt_pk_bf16_f32 v79, v22, v23\n"
      "v_pk_mul_f32 v[92:93], v[92:93], s[52:53]\n"
      "v_lshlrev_b32_e32 v20, 16, v81\n"
      "v_and_b32_e32 v21, 0xffff0000, v81\n"
      "v_accvgpr_write_b32 a7, v79\n"
      "v_cvt_pk_bf16_f32 v80, v92, v93\n"
      "v_pk_mul_f32 v[20:21], v[20:21], s[52:53]\n"
      "v_lshlrev_b32_e32 v22, 16, v82\n"
      "v_and_b32_e32 v23, 0xffff0000, v82\n"
      "v_accvgpr_write_b32 a8, v80\n"
      "v_cvt_pk_bf16_f32 v81, v20, v21\n"
      "v_pk_mul_f32 v[22:23], v[22:23], s[52:53]\n"
      "v_lshlrev_b32_e32 v92, 16, v83\n"
      "v_and_b32_e32 v93, 0xffff0000, v83\n"
      "v_accvgpr_write_b32 a9, v81\n"
      "v_cvt_pk_bf16_f32 v82, v22, v23\n"
      "v_pk_mul_f32 v[92:93], v[92:93], s[52:53]\n"
      "s_waitcnt lgkmcnt(0)\n"
      "v_lshlrev_b32_e32 v20, 16, v84\n"
      "v_and_b32_e32 v21, 0xffff0000, v84\n"
      "v_accvgpr_write_b32 a10, v82\n"
      "v_cvt_pk_bf16_f32 v83, v92, v93\n"
      "v_pk_mul_f32 v[20:21], v[20:21], s[52:53]\n"
      "v_lshlrev_b32_e32 v22, 16, v85\n"
      "v_and_b32_e32 v23, 0xffff0000, v85\n"
      "v_accvgpr_write_b32 a11, v83\n"
      "v_cvt_pk_bf16_f32 v84, v20, v21\n"
      "v_pk_mul_f32 v[22:23], v[22:23], s[52:53]\n"
      "v_lshlrev_b32_e32 v92, 16, v86\n"
      "v_and_b32_e32 v93, 0xffff0000, v86\n"
      "v_accvgpr_write_b32 a12, v84\n"
      "v_cvt_pk_bf16_f32 v85, v22, v23\n"
      "v_pk_mul_f32 v[92:93], v[92:93], s[52:53]\n"
      "v_lshlrev_b32_e32 v20, 16, v87\n"
      "v_and_b32_e32 v21, 0xffff0000, v87\n"
      "v_accvgpr_write_b32 a13, v85\n"
      "v_cvt_pk_bf16_f32 v86, v92, v93\n"
      "v_pk_mul_f32 v[20:21], v[20:21], s[52:53]\n"
      "v_accvgpr_write_b32 a14, v86\n"
      "v_cvt_pk_bf16_f32 v87, v20, v21\n"
      "v_accvgpr_write_b32 a15, v87\n"
      "v_mfma_f32_16x16x32_bf16 v[24:27], a[96:99], a[0:3], 0\n"
      "s_add_u32 m0, s50, 32768\n"
      "s_mov_b32 s38, 8192\n"
      "buffer_load_dwordx4 v9, s[16:19], s38 offen nt lds\n"
      "v_mfma_f32_16x16x32_bf16 v[24:27], a[100:103], a[4:7], v[24:27]\n"
      "v_mfma_f32_16x16x32_bf16 v[24:27], a[104:107], a[8:11], v[24:27]\n"
      "s_waitcnt vmcnt(5)\n"
      "s_barrier\n"
      "s_add_u32 m0, s50, 33664\n"
      "s_nop 0\n"
      "buffer_load_dwordx4 v9, s[16:19], s38 offen offset:128 nt lds\n"
      "s_add_u32 m0, s50, 34816\n"
      "s_nop 0\n"
      "buffer_load_dwordx4 v10, s[16:19], s38 offen nt lds\n"
      "s_add_u32 m0, s50, 35712\n"
      "s_nop 0\n"
      "buffer_load_dwordx4 v10, s[16:19], s38 offen offset:128 nt lds\n"
      "s_add_u32 m0, s50, 49152\n"
      "s_mov_b32 s38, 12288\n"
      "buffer_load_dwordx4 v9, s[16:19], s38 offen nt lds\n"
      "s_add_u32 m0, s50, 50048\n"
      "s_nop 0\n"
      "buffer_load_dwordx4 v9, s[16:19], s38 offen offset:128 nt lds\n"
      "v_mfma_f32_16x16x32_bf16 v[24:27], a[108:111], a[12:15], v[24:27]\n"
      "ds_read_b64_tr_b16 a[160:161], v3 offset:0\n"
      "v_mfma_f32_16x16x32_bf16 v[28:31], a[112:115], a[0:3], 0\n"
      "ds_read_b64_tr_b16 a[162:163], v3 offset:4096\n"
      "s_add_u32 m0, s50, 51200\n"
      "s_nop 0\n"
      "buffer_load_dwordx4 v10, s[16:19], s38 offen nt lds\n"
      "s_add_u32 m0, s50, 52096\n"
      "s_nop 0\n"
      "buffer_load_dwordx4 v10, s[16:19], s38 offen offset:128 nt lds\n"
      "s_add_u32 m0, s40, 8192\n"
      "s_mov_b32 s38, 8192\n"
      "buffer_load_dwordx4 v7, s[20:23], s38 offen nt lds\n"
      "s_add_u32 m0, s40, 9088\n"
      "s_nop 0\n"
      "buffer_load_dwordx4 v7, s[20:23], s38 offen offset:128 nt lds\n"
      "s_waitcnt vmcnt(10)\n"
      "ds_read_b128 v[72:75], v11 offset:4096\n"
      "ds_read_b128 v[76:79], v12 offset:4096\n"
      "ds_read_b128 v[80:83], v11 offset:5120\n"
      "ds_read_b128 v[84:87], v12 offset:5120\n"
      "v_mfma_f32_16x16x32_bf16 v[28:31], a[116:119], a[4:7], v[28:31]\n"
      "ds_read_b64_tr_b16 a[164:165], v4 offset:0\n"
      "v_mfma_f32_16x16x32_bf16 v[28:31], a[120:123], a[8:11], v[28:31]\n"
      "ds_read_b64_tr_b16 a[166:167], v4 offset:4096\n"
      "s_waitcnt lgkmcnt(5)\n"
      "v_lshlrev_b32_e32 v20, 16, v72\n"
      "v_and_b32_e32 v21, 0xffff0000, v72\n"
      "v_pk_mul_f32 v[20:21], v[20:21], s[52:53]\n"
      "v_lshlrev_b32_e32 v22, 16, v73\n"
      "v_and_b32_e32 v23, 0xffff0000, v73\n"
      "v_cvt_pk_bf16_f32 v72, v20, v21\n"
      "v_pk_mul_f32 v[22:23], v[22:23], s[52:53]\n"
      "v_lshlrev_b32_e32 v92, 16, v74\n"
      "v_mfma_f32_16x16x32_bf16 v[28:31], a[124:127], a[12:15], v[28:31]\n"
      "ds_read_b64_tr_b16 a[168:169], v5 offset:0\n"
      "v_and_b32_e32 v93, 0xffff0000, v74\n"
      "v_accvgpr_write_b32 a16, v72\n"
      "v_cvt_pk_bf16_f32 v73, v22, v23\n"
      "v_pk_mul_f32 v[92:93], v[92:93], s[52:53]\n"
      "v_lshlrev_b32_e32 v20, 16, v75\n"
      "v_and_b32_e32 v21, 0xffff0000, v75\n"
      "v_accvgpr_write_b32 a17, v73\n"
      "v_cvt_pk_bf16_f32 v74, v92, v93\n"
      "ds_read_b64_tr_b16 a[170:171], v5 offset:4096\n"
      "ds_read_b64_tr_b16 a[172:173], v6 offset:0\n"
      "v_pk_mul_f32 v[20:21], v[20:21], s[52:53]\n"
      "s_waitcnt lgkmcnt(7)\n"
      "v_lshlrev_b32_e32 v22, 16, v76\n"
      "v_and_b32_e32 v23, 0xffff0000, v76\n"
      "v_accvgpr_write_b32 a18, v74\n"
      "v_cvt_pk_bf16_f32 v75, v20, v21\n"
      "v_pk_mul_f32 v[22:23], v[22:23], s[52:53]\n"
      "v_lshlrev_b32_e32 v92, 16, v77\n"
      "v_and_b32_e32 v93, 0xffff0000, v77\n"
      "ds_read_b64_tr_b16 a[174:175], v6 offset:4096\n"
      "ds_read_b64_tr_b16 a[176:177], v3 offset:1024\n"
      "v_max3_f32 v94, v24, v25, v26\n"
      "v_max3_f32 v94, v94, v27, v28\n"
      "v_max3_f32 v94, v94, v29, v30\n"
      "v_max_f32_e32 v94, v94, v31\n"
      "v_accvgpr_write_b32 a19, v75\n"
      "v_cvt_pk_bf16_f32 v76, v22, v23\n"
      "v_pk_mul_f32 v[92:93], v[92:93], s[52:53]\n"
      "v_lshlrev_b32_e32 v20, 16, v78\n"
      "v_and_b32_e32 v21, 0xffff0000, v78\n"
      "v_accvgpr_write_b32 a20, v76\n"
      "v_cvt_pk_bf16_f32 v77, v92, v93\n"
      "v_pk_mul_f32 v[20:21], v[20:21], s[52:53]\n"
      "ds_read_b64_tr_b16 a[178:179], v3 offset:5120\n"
      "ds_read_b64_tr_b16 a[180:181], v4 offset:1024\n"
      "v_mov_b32_e32 v91, v94\n"
      "s_nop 1\n"
      "v_permlane16_swap_b32_e32 v91, v94\n"
      "v_max_f32_e32 v94, v91, v94\n"
      "v_mov_b32_e32 v91, v94\n"
      "v_lshlrev_b32_e32 v22, 16, v79\n"
      "v_and_b32_e32 v23, 0xffff0000, v79\n"
      "v_accvgpr_write_b32 a21, v77\n"
      "v_cvt_pk_bf16_f32 v78, v20, v21\n"
      "v_pk_mul_f32 v[22:23], v[22:23], s[52:53]\n"
      "s_waitcnt lgkmcnt(8)\n"
      "v_lshlrev_b32_e32 v92, 16, v80\n"
      "v_and_b32_e32 v93, 0xffff0000, v80\n"
      "v_accvgpr_write_b32 a22, v78\n"
      "ds_read_b64_tr_b16 a[182:183], v4 offset:5120\n"
      "v_permlane32_swap_b32_e32 v91, v94\n"
      "v_max_f32_e32 v94, v91, v94\n"
      "v_xor_b32_e32 v48, 0x80000000, v94\n"
      "v_xor_b32_e32 v49, 0x80000000, v94\n"
      "v_cvt_pk_bf16_f32 v79, v22, v23\n"
      "v_pk_mul_f32 v[92:93], v[92:93], s[52:53]\n"
      "v_lshlrev_b32_e32 v20, 16, v81\n"
      "v_and_b32_e32 v21, 0xffff0000, v81\n"
      "v_accvgpr_write_b32 a23, v79\n"
      "v_cvt_pk_bf16_f32 v80, v92, v93\n"
      "v_pk_mul_f32 v[20:21], v[20:21], s[52:53]\n"
      "v_lshlrev_b32_e32 v22, 16, v82\n"
      "ds_read_b64_tr_b16 a[184:185], v5 offset:1024\n"
      "v_xor_b32_e32 v50, 0x80000000, v94\n"
      "v_xor_b32_e32 v51, 0x80000000, v94\n"
      "v_sub_f32_e32 v24, v24, v94\n"
      "v_sub_f32_e32 v25, v25, v94\n"
      "v_and_b32_e32 v23, 0xffff0000, v82\n"
      "v_accvgpr_write_b32 a24, v80\n"
      "v_cvt_pk_bf16_f32 v81, v20, v21\n"
      "v_pk_mul_f32 v[22:23], v[22:23], s[52:53]\n"
      "v_lshlrev_b32_e32 v92, 16, v83\n"
      "v_and_b32_e32 v93, 0xffff0000, v83\n"
      "v_accvgpr_write_b32 a25, v81\n"
      "v_cvt_pk_bf16_f32 v82, v22, v23\n"
      "ds_read_b64_tr_b16 a[186:187], v5 offset:5120\n"
      "v_sub_f32_e32 v26, v26, v94\n"
      "v_sub_f32_e32 v27, v27, v94\n"
      "v_sub_f32_e32 v28, v28, v94\n"
      "v_sub_f32_e32 v29, v29, v94\n"
      "v_pk_mul_f32 v[92:93], v[92:93], s[52:53]\n"
      "v_lshlrev_b32_e32 v20, 16, v84\n"
      "v_and_b32_e32 v21, 0xffff0000, v84\n"
      "v_accvgpr_write_b32 a26, v82\n"
      "v_cvt_pk_bf16_f32 v83, v92, v93\n"
      "v_pk_mul_f32 v[20:21], v[20:21], s[52:53]\n"
      "v_lshlrev_b32_e32 v22, 16, v85\n"
      "v_and_b32_e32 v23, 0xffff0000, v85\n"
      "ds_read_b64_tr_b16 a[188:189], v6 offset:1024\n"
      "v_sub_f32_e32 v30, v30, v94\n"
      "v_sub_f32_e32 v31, v31, v94\n"
      "v_accvgpr_write_b32 a27, v83\n"
      "v_cvt_pk_bf16_f32 v84, v20, v21\n"
      "v_pk_mul_f32 v[22:23], v[22:23], s[52:53]\n"
      "v_lshlrev_b32_e32 v92, 16, v86\n"
      "v_and_b32_e32 v93, 0xffff0000, v86\n"
      "v_accvgpr_write_b32 a28, v84\n"
      "v_cvt_pk_bf16_f32 v85, v22, v23\n"
      "v_pk_mul_f32 v[92:93], v[92:93], s[52:53]\n"
      "ds_read_b64_tr_b16 a[190:191], v6 offset:5120\n"
      "v_exp_f32_e32 v24, v24\n"
      "v_exp_f32_e32 v25, v25\n"
      "v_lshlrev_b32_e32 v20, 16, v87\n"
      "v_and_b32_e32 v21, 0xffff0000, v87\n"
      "v_accvgpr_write_b32 a29, v85\n"
      "v_cvt_pk_bf16_f32 v86, v92, v93\n"
      "v_pk_mul_f32 v[20:21], v[20:21], s[52:53]\n"
      "v_accvgpr_write_b32 a30, v86\n"
      "v_cvt_pk_bf16_f32 v87, v20, v21\n"
      "v_accvgpr_write_b32 a31, v87\n"
      "v_mfma_f32_16x16x32_bf16 v[32:35], a[96:99], a[16:19], 0\n"
      "v_exp_f32_e32 v26, v26\n"
      "v_add_f32_e32 v13, v24, v13\n"
      "v_add_f32_e32 v13, v25, v13\n"
      "v_cvt_pk_bf16_f32 v40, v24, v25\n"
      "v_mfma_f32_16x16x32_bf16 v[32:35], a[100:103], a[20:23], v[32:35]\n"
      "v_exp_f32_e32 v27, v27\n"
      "v_add_f32_e32 v13, v26, v13\n"
      "v_mfma_f32_16x16x32_bf16 v[32:35], a[104:107], a[24:27], v[32:35]\n"
      "v_exp_f32_e32 v28, v28\n"
      "v_exp_f32_e32 v29, v29\n"
      "v_add_f32_e32 v13, v27, v13\n"
      "v_cvt_pk_bf16_f32 v41, v26, v27\n"
      "v_mfma_f32_16x16x32_bf16 v[32:35], a[108:111], a[28:31], v[32:35]\n"
      "v_exp_f32_e32 v30, v30\n"
      "v_add_f32_e32 v13, v28, v13\n"
      "v_add_f32_e32 v13, v29, v13\n"
      "v_cvt_pk_bf16_f32 v42, v28, v29\n"
      "v_mfma_f32_16x16x32_bf16 v[36:39], a[112:115], a[16:19], 0\n"
      "s_add_u32 m0, s40, 73728\n"
      "s_nop 0\n"
      "buffer_load_dwordx4 v8, s[24:27], s38 offen nt lds\n"
      "s_add_u32 m0, s40, 74624\n"
      "s_nop 0\n"
      "buffer_load_dwordx4 v8, s[24:27], s38 offen offset:128 nt lds\n"
      "s_add_u32 m0, s50, 98304\n"
      "s_mov_b32 s38, 16384\n"
      "buffer_load_dwordx4 v9, s[16:19], s38 offen nt lds\n"
      "s_add_u32 m0, s50, 99200\n"
      "s_nop 0\n"
      "buffer_load_dwordx4 v9, s[16:19], s38 offen offset:128 nt lds\n"
      "v_exp_f32_e32 v31, v31\n"
      "v_add_f32_e32 v13, v30, v13\n"
      "v_add_u32_e32 v11, s51, v1\n"
      "v_add_u32_e32 v12, s51, v2\n"
      "s_waitcnt vmcnt(10)\n"
      "ds_read_b128 v[72:75], v11 offset:32768\n"
      "ds_read_b128 v[76:79], v12 offset:32768\n"
      "ds_read_b128 v[80:83], v11 offset:33792\n"
      "ds_read_b128 v[84:87], v12 offset:33792\n"
      "v_mfma_f32_16x16x32_bf16 v[36:39], a[116:119], a[20:23], v[36:39]\n"
      "v_add_f32_e32 v13, v31, v13\n"
      "v_cvt_pk_bf16_f32 v43, v30, v31\n"
      "v_mfma_f32_16x16x32_bf16 v[36:39], a[120:123], a[24:27], v[36:39]\n"
      "s_waitcnt lgkmcnt(3)\n"
      "v_lshlrev_b32_e32 v20, 16, v72\n"
      "v_and_b32_e32 v21, 0xffff0000, v72\n"
      "v_pk_mul_f32 v[20:21], v[20:21], s[52:53]\n"
      "v_lshlrev_b32_e32 v22, 16, v73\n"
      "v_and_b32_e32 v23, 0xffff0000, v73\n"
      "v_cvt_pk_bf16_f32 v72, v20, v21\n"
      "v_pk_mul_f32 v[22:23], v[22:23], s[52:53]\n"
      "v_lshlrev_b32_e32 v92, 16, v74\n"
      "v_mfma_f32_16x16x32_bf16 v[36:39], a[124:127], a[28:31], v[36:39]\n"
      "v_and_b32_e32 v93, 0xffff0000, v74\n"
      "v_accvgpr_write_b32 a32, v72\n"
      "v_cvt_pk_bf16_f32 v73, v22, v23\n"
      "v_pk_mul_f32 v[92:93], v[92:93], s[52:53]\n"
      "v_lshlrev_b32_e32 v20, 16, v75\n"
      "v_and_b32_e32 v21, 0xffff0000, v75\n"
      "v_accvgpr_write_b32 a33, v73\n"
      "v_cvt_pk_bf16_f32 v74, v92, v93\n"
      "v_mfma_f32_16x16x32_bf16 a[224:227], a[160:163], v[40:43], 0\n"
      "v_pk_mul_f32 v[20:21], v[20:21], s[52:53]\n"
      "s_waitcnt lgkmcnt(2)\n"
      "v_lshlrev_b32_e32 v22, 16, v76\n"
      "v_and_b32_e32 v23, 0xffff0000, v76\n"
      "v_accvgpr_write_b32 a34, v74\n"
      "v_cvt_pk_bf16_f32 v75, v20, v21\n"
      "v_pk_mul_f32 v[22:23], v[22:23], s[52:53]\n"
      "v_lshlrev_b32_e32 v92, 16, v77\n"
      "v_and_b32_e32 v93, 0xffff0000, v77\n"
      "v_mfma_f32_16x16x32_bf16 a[228:231], a[164:167], v[40:43], 0\n"
      "v_max3_f32 v94, v32, v33, v34\n"
      "v_max3_f32 v94, v94, v35, v36\n"
      "v_max3_f32 v94, v94, v37, v38\n"
      "v_max_f32_e32 v94, v94, v39\n"
      "v_accvgpr_write_b32 a35, v75\n"
      "v_cvt_pk_bf16_f32 v76, v22, v23\n"
      "v_pk_mul_f32 v[92:93], v[92:93], s[52:53]\n"
      "v_lshlrev_b32_e32 v20, 16, v78\n"
      "v_and_b32_e32 v21, 0xffff0000, v78\n"
      "v_accvgpr_write_b32 a36, v76\n"
      "v_cvt_pk_bf16_f32 v77, v92, v93\n"
      "v_pk_mul_f32 v[20:21], v[20:21], s[52:53]\n"
      "v_mfma_f32_16x16x32_bf16 a[232:235], a[168:171], v[40:43], 0\n"
      "v_mov_b32_e32 v91, v94\n"
      "s_nop 1\n"
      "v_permlane16_swap_b32_e32 v91, v94\n"
      "v_max_f32_e32 v94, v91, v94\n"
      "v_mov_b32_e32 v91, v94\n"
      "v_lshlrev_b32_e32 v22, 16, v79\n"
      "v_and_b32_e32 v23, 0xffff0000, v79\n"
      "v_accvgpr_write_b32 a37, v77\n"
      "v_cvt_pk_bf16_f32 v78, v20, v21\n"
      "v_pk_mul_f32 v[22:23], v[22:23], s[52:53]\n"
      "s_waitcnt lgkmcnt(0)\n"
      "v_lshlrev_b32_e32 v92, 16, v80\n"
      "v_and_b32_e32 v93, 0xffff0000, v80\n"
      "v_accvgpr_write_b32 a38, v78\n"
      "v_mfma_f32_16x16x32_bf16 a[236:239], a[172:175], v[40:43], 0\n"
      "v_permlane32_swap_b32_e32 v91, v94\n"
      "v_max_f32_e32 v94, v91, v94\n"
      "v_xor_b32_e32 v52, 0x80000000, v94\n"
      "v_xor_b32_e32 v53, 0x80000000, v94\n"
      "v_cvt_pk_bf16_f32 v79, v22, v23\n"
      "v_pk_mul_f32 v[92:93], v[92:93], s[52:53]\n"
      "v_lshlrev_b32_e32 v20, 16, v81\n"
      "v_and_b32_e32 v21, 0xffff0000, v81\n"
      "v_accvgpr_write_b32 a39, v79\n"
      "v_cvt_pk_bf16_f32 v80, v92, v93\n"
      "v_pk_mul_f32 v[20:21], v[20:21], s[52:53]\n"
      "v_lshlrev_b32_e32 v22, 16, v82\n"
      "v_mfma_f32_16x16x32_bf16 a[240:243], a[176:179], v[40:43], 0\n"
      "v_xor_b32_e32 v54, 0x80000000, v94\n"
      "v_xor_b32_e32 v55, 0x80000000, v94\n"
      "v_sub_f32_e32 v32, v32, v94\n"
      "v_sub_f32_e32 v33, v33, v94\n"
      "v_and_b32_e32 v23, 0xffff0000, v82\n"
      "v_accvgpr_write_b32 a40, v80\n"
      "v_cvt_pk_bf16_f32 v81, v20, v21\n"
      "v_pk_mul_f32 v[22:23], v[22:23], s[52:53]\n"
      "v_lshlrev_b32_e32 v92, 16, v83\n"
      "v_and_b32_e32 v93, 0xffff0000, v83\n"
      "v_accvgpr_write_b32 a41, v81\n"
      "v_cvt_pk_bf16_f32 v82, v22, v23\n"
      "v_mfma_f32_16x16x32_bf16 a[244:247], a[180:183], v[40:43], 0\n"
      "v_sub_f32_e32 v34, v34, v94\n"
      "v_sub_f32_e32 v35, v35, v94\n"
      "v_sub_f32_e32 v36, v36, v94\n"
      "v_sub_f32_e32 v37, v37, v94\n"
      "v_pk_mul_f32 v[92:93], v[92:93], s[52:53]\n"
      "v_lshlrev_b32_e32 v20, 16, v84\n"
      "v_and_b32_e32 v21, 0xffff0000, v84\n"
      "v_accvgpr_write_b32 a42, v82\n"
      "v_cvt_pk_bf16_f32 v83, v92, v93\n"
      "v_pk_mul_f32 v[20:21], v[20:21], s[52:53]\n"
      "v_lshlrev_b32_e32 v22, 16, v85\n"
      "v_and_b32_e32 v23, 0xffff0000, v85\n"
      "v_mfma_f32_16x16x32_bf16 a[248:251], a[184:187], v[40:43], 0\n"
      "v_sub_f32_e32 v38, v38, v94\n"
      "v_sub_f32_e32 v39, v39, v94\n"
      "v_accvgpr_write_b32 a43, v83\n"
      "v_cvt_pk_bf16_f32 v84, v20, v21\n"
      "v_pk_mul_f32 v[22:23], v[22:23], s[52:53]\n"
      "v_lshlrev_b32_e32 v92, 16, v86\n"
      "v_and_b32_e32 v93, 0xffff0000, v86\n"
      "v_accvgpr_write_b32 a44, v84\n"
      "v_cvt_pk_bf16_f32 v85, v22, v23\n"
      "v_pk_mul_f32 v[92:93], v[92:93], s[52:53]\n"
      "v_mfma_f32_16x16x32_bf16 a[252:255], a[188:191], v[40:43], 0\n"
      "v_exp_f32_e32 v32, v32\n"
      "v_exp_f32_e32 v33, v33\n"
      "v_lshlrev_b32_e32 v20, 16, v87\n"
      "v_and_b32_e32 v21, 0xffff0000, v87\n"
      "v_accvgpr_write_b32 a45, v85\n"
      "v_cvt_pk_bf16_f32 v86, v92, v93\n"
      "v_pk_mul_f32 v[20:21], v[20:21], s[52:53]\n"
      "v_accvgpr_write_b32 a46, v86\n"
      "v_cvt_pk_bf16_f32 v87, v20, v21\n"
      "v_accvgpr_write_b32 a47, v87\n"
      "v_mfma_f32_16x16x32_bf16 v[24:27], a[96:99], a[32:35], 0\n"
      "v_exp_f32_e32 v34, v34\n"
      "v_add_f32_e32 v14, v32, v14\n"
      "v_add_f32_e32 v14, v33, v14\n"
      "v_cvt_pk_bf16_f32 v44, v32, v33\n"
      "v_mfma_f32_16x16x32_bf16 v[24:27], a[100:103], a[36:39], v[24:27]\n"
      "v_exp_f32_e32 v35, v35\n"
      "v_add_f32_e32 v14, v34, v14\n"
      "v_mfma_f32_16x16x32_bf16 v[24:27], a[104:107], a[40:43], v[24:27]\n"
      "s_add_u32 m0, s50, 100352\n"
      "s_nop 0\n"
      "buffer_load_dwordx4 v10, s[16:19], s38 offen nt lds\n"
      "v_exp_f32_e32 v36, v36\n"
      "v_exp_f32_e32 v37, v37\n"
      "v_add_f32_e32 v14, v35, v14\n"
      "v_cvt_pk_bf16_f32 v45, v34, v35\n"
      "v_mfma_f32_16x16x32_bf16 v[24:27], a[108:111], a[44:47], v[24:27]\n"
      "v_exp_f32_e32 v38, v38\n"
      "v_add_f32_e32 v14, v36, v14\n"
      "v_add_f32_e32 v14, v37, v14\n"
      "v_cvt_pk_bf16_f32 v46, v36, v37\n"
      "v_mfma_f32_16x16x32_bf16 v[28:31], a[112:115], a[32:35], 0\n"
      "s_add_u32 m0, s50, 101248\n"
      "s_nop 0\n"
      "buffer_load_dwordx4 v10, s[16:19], s38 offen offset:128 nt lds\n"
      "s_add_u32 m0, s50, 114688\n"
      "s_mov_b32 s38, 20480\n"
      "buffer_load_dwordx4 v9, s[16:19], s38 offen nt lds\n"
      "s_add_u32 m0, s50, 115584\n"
      "s_nop 0\n"
      "buffer_load_dwordx4 v9, s[16:19], s38 offen offset:128 nt lds\n"
      "v_exp_f32_e32 v39, v39\n"
      "v_add_f32_e32 v14, v38, v14\n"
      "s_waitcnt vmcnt(10)\n"
      "ds_read_b128 v[72:75], v11 offset:49152\n"
      "ds_read_b128 v[76:79], v12 offset:49152\n"
      "ds_read_b128 v[80:83], v11 offset:50176\n"
      "ds_read_b128 v[84:87], v12 offset:50176\n"
      "v_mfma_f32_16x16x32_bf16 v[28:31], a[116:119], a[36:39], v[28:31]\n"
      "v_add_f32_e32 v14, v39, v14\n"
      "v_cvt_pk_bf16_f32 v47, v38, v39\n"
      "v_mfma_f32_16x16x32_bf16 v[28:31], a[120:123], a[40:43], v[28:31]\n"
      "s_waitcnt lgkmcnt(3)\n"
      "v_lshlrev_b32_e32 v20, 16, v72\n"
      "v_and_b32_e32 v21, 0xffff0000, v72\n"
      "v_pk_mul_f32 v[20:21], v[20:21], s[52:53]\n"
      "v_lshlrev_b32_e32 v22, 16, v73\n"
      "v_and_b32_e32 v23, 0xffff0000, v73\n"
      "v_cvt_pk_bf16_f32 v72, v20, v21\n"
      "v_pk_mul_f32 v[22:23], v[22:23], s[52:53]\n"
      "v_lshlrev_b32_e32 v92, 16, v74\n"
      "v_mfma_f32_16x16x32_bf16 v[28:31], a[124:127], a[44:47], v[28:31]\n"
      "v_and_b32_e32 v93, 0xffff0000, v74\n"
      "v_accvgpr_write_b32 a48, v72\n"
      "v_cvt_pk_bf16_f32 v73, v22, v23\n"
      "v_pk_mul_f32 v[92:93], v[92:93], s[52:53]\n"
      "v_lshlrev_b32_e32 v20, 16, v75\n"
      "v_and_b32_e32 v21, 0xffff0000, v75\n"
      "v_accvgpr_write_b32 a49, v73\n"
      "v_cvt_pk_bf16_f32 v74, v92, v93\n"
      "v_mfma_f32_16x16x32_bf16 v[96:99], a[160:163], v[44:47], 0\n"
      "v_pk_mul_f32 v[20:21], v[20:21], s[52:53]\n"
      "s_waitcnt lgkmcnt(2)\n"
      "v_lshlrev_b32_e32 v22, 16, v76\n"
      "v_and_b32_e32 v23, 0xffff0000, v76\n"
      "v_accvgpr_write_b32 a50, v74\n"
      "v_cvt_pk_bf16_f32 v75, v20, v21\n"
      "v_pk_mul_f32 v[22:23], v[22:23], s[52:53]\n"
      "v_lshlrev_b32_e32 v92, 16, v77\n"
      "v_and_b32_e32 v93, 0xffff0000, v77\n"
      "v_mfma_f32_16x16x32_bf16 v[100:103], a[164:167], v[44:47], 0\n"
      "v_max3_f32 v94, v24, v25, v26\n"
      "v_max3_f32 v94, v94, v27, v28\n"
      "v_max3_f32 v94, v94, v29, v30\n"
      "v_max_f32_e32 v94, v94, v31\n"
      "v_accvgpr_write_b32 a51, v75\n"
      "v_cvt_pk_bf16_f32 v76, v22, v23\n"
      "v_pk_mul_f32 v[92:93], v[92:93], s[52:53]\n"
      "v_lshlrev_b32_e32 v20, 16, v78\n"
      "v_and_b32_e32 v21, 0xffff0000, v78\n"
      "v_accvgpr_write_b32 a52, v76\n"
      "v_cvt_pk_bf16_f32 v77, v92, v93\n"
      "v_pk_mul_f32 v[20:21], v[20:21], s[52:53]\n"
      "v_mfma_f32_16x16x32_bf16 v[104:107], a[168:171], v[44:47], 0\n"
      "v_mov_b32_e32 v91, v94\n"
      "s_nop 1\n"
      "v_permlane16_swap_b32_e32 v91, v94\n"
      "v_max_f32_e32 v94, v91, v94\n"
      "v_mov_b32_e32 v91, v94\n"
      "v_lshlrev_b32_e32 v22, 16, v79\n"
      "v_and_b32_e32 v23, 0xffff0000, v79\n"
      "v_accvgpr_write_b32 a53, v77\n"
      "v_cvt_pk_bf16_f32 v78, v20, v21\n"
      "v_pk_mul_f32 v[22:23], v[22:23], s[52:53]\n"
      "s_waitcnt lgkmcnt(0)\n"
      "v_lshlrev_b32_e32 v92, 16, v80\n"
      "v_and_b32_e32 v93, 0xffff0000, v80\n"
      "v_accvgpr_write_b32 a54, v78\n"
      "v_mfma_f32_16x16x32_bf16 v[108:111], a[172:175], v[44:47], 0\n"
      "v_permlane32_swap_b32_e32 v91, v94\n"
      "v_max_f32_e32 v94, v91, v94\n"
      "v_xor_b32_e32 v56, 0x80000000, v94\n"
      "v_xor_b32_e32 v57, 0x80000000, v94\n"
      "v_cvt_pk_bf16_f32 v79, v22, v23\n"
      "v_pk_mul_f32 v[92:93], v[92:93], s[52:53]\n"
      "v_lshlrev_b32_e32 v20, 16, v81\n"
      "v_and_b32_e32 v21, 0xffff0000, v81\n"
      "v_accvgpr_write_b32 a55, v79\n"
      "v_cvt_pk_bf16_f32 v80, v92, v93\n"
      "v_pk_mul_f32 v[20:21], v[20:21], s[52:53]\n"
      "v_lshlrev_b32_e32 v22, 16, v82\n"
      "v_mfma_f32_16x16x32_bf16 v[112:115], a[176:179], v[44:47], 0\n"
      "v_xor_b32_e32 v58, 0x80000000, v94\n"
      "v_xor_b32_e32 v59, 0x80000000, v94\n"
      "v_sub_f32_e32 v24, v24, v94\n"
      "v_sub_f32_e32 v25, v25, v94\n"
      "v_and_b32_e32 v23, 0xffff0000, v82\n"
      "v_accvgpr_write_b32 a56, v80\n"
      "v_cvt_pk_bf16_f32 v81, v20, v21\n"
      "v_pk_mul_f32 v[22:23], v[22:23], s[52:53]\n"
      "v_lshlrev_b32_e32 v92, 16, v83\n"
      "v_and_b32_e32 v93, 0xffff0000, v83\n"
      "v_accvgpr_write_b32 a57, v81\n"
      "v_cvt_pk_bf16_f32 v82, v22, v23\n"
      "v_mfma_f32_16x16x32_bf16 v[116:119], a[180:183], v[44:47], 0\n"
      "v_sub_f32_e32 v26, v26, v94\n"
      "v_sub_f32_e32 v27, v27, v94\n"
      "v_sub_f32_e32 v28, v28, v94\n"
      "v_sub_f32_e32 v29, v29, v94\n"
      "v_pk_mul_f32 v[92:93], v[92:93], s[52:53]\n"
      "v_lshlrev_b32_e32 v20, 16, v84\n"
      "v_and_b32_e32 v21, 0xffff0000, v84\n"
      "v_accvgpr_write_b32 a58, v82\n"
      "v_cvt_pk_bf16_f32 v83, v92, v93\n"
      "v_pk_mul_f32 v[20:21], v[20:21], s[52:53]\n"
      "v_lshlrev_b32_e32 v22, 16, v85\n"
      "v_and_b32_e32 v23, 0xffff0000, v85\n"
      "v_mfma_f32_16x16x32_bf16 v[120:123], a[184:187], v[44:47], 0\n"
      "v_sub_f32_e32 v30, v30, v94\n"
      "v_sub_f32_e32 v31, v31, v94\n"
      "v_accvgpr_write_b32 a59, v83\n"
      "v_cvt_pk_bf16_f32 v84, v20, v21\n"
      "v_pk_mul_f32 v[22:23], v[22:23], s[52:53]\n"
      "v_lshlrev_b32_e32 v92, 16, v86\n"
      "v_and_b32_e32 v93, 0xffff0000, v86\n"
      "v_accvgpr_write_b32 a60, v84\n"
      "v_cvt_pk_bf16_f32 v85, v22, v23\n"
      "v_pk_mul_f32 v[92:93], v[92:93], s[52:53]\n"
      "v_mfma_f32_16x16x32_bf16 v[124:127], a[188:191], v[44:47], 0\n"
      "v_exp_f32_e32 v24, v24\n"
      "v_exp_f32_e32 v25, v25\n"
      "v_lshlrev_b32_e32 v20, 16, v87\n"
      "v_and_b32_e32 v21, 0xffff0000, v87\n"
      "v_accvgpr_write_b32 a61, v85\n"
      "v_cvt_pk_bf16_f32 v86, v92, v93\n"
      "v_pk_mul_f32 v[20:21], v[20:21], s[52:53]\n"
      "v_accvgpr_write_b32 a62, v86\n"
      "v_cvt_pk_bf16_f32 v87, v20, v21\n"
      "v_accvgpr_write_b32 a63, v87\n"
      "v_mfma_f32_16x16x32_bf16 v[32:35], a[96:99], a[48:51], 0\n"
      "v_exp_f32_e32 v26, v26\n"
      "v_add_f32_e32 v15, v24, v15\n"
      "v_add_f32_e32 v15, v25, v15\n"
      "v_cvt_pk_bf16_f32 v40, v24, v25\n"
      "v_mfma_f32_16x16x32_bf16 v[32:35], a[100:103], a[52:55], v[32:35]\n"
      "v_exp_f32_e32 v27, v27\n"
      "v_add_f32_e32 v15, v26, v15\n"
      "v_mfma_f32_16x16x32_bf16 v[32:35], a[104:107], a[56:59], v[32:35]\n"
      "v_exp_f32_e32 v28, v28\n"
      "v_exp_f32_e32 v29, v29\n"
      "v_add_f32_e32 v15, v27, v15\n"
      "v_cvt_pk_bf16_f32 v41, v26, v27\n"
      "v_mfma_f32_16x16x32_bf16 v[32:35], a[108:111], a[60:63], v[32:35]\n"
      "v_exp_f32_e32 v30, v30\n"
      "v_add_f32_e32 v15, v28, v15\n"
      "v_add_f32_e32 v15, v29, v15\n"
      "v_cvt_pk_bf16_f32 v42, v28, v29\n"
      "v_mfma_f32_16x16x32_bf16 v[36:39], a[112:115], a[48:51], 0\n"
      "s_add_u32 m0, s50, 116736\n"
      "s_nop 0\n"
      "buffer_load_dwordx4 v10, s[16:19], s38 offen nt lds\n"
      "v_exp_f32_e32 v31, v31\n"
      "v_add_f32_e32 v15, v30, v15\n"
      "v_mfma_f32_16x16x32_bf16 v[36:39], a[116:119], a[52:55], v[36:39]\n"
      "s_waitcnt vmcnt(9)\n"
      "s_barrier\n"
      "s_add_u32 m0, s50, 117632\n"
      "s_nop 0\n"
      "buffer_load_dwordx4 v10, s[16:19], s38 offen offset:128 nt lds\n"
      "v_add_f32_e32 v15, v31, v15\n"
      "v_cvt_pk_bf16_f32 v43, v30, v31\n"
      "v_mfma_f32_16x16x32_bf16 v[36:39], a[120:123], a[56:59], v[36:39]\n"
      "ds_read_b128 a[128:131], v1 offset:8192\n"
      "ds_read_b128 a[132:135], v2 offset:8192\n"
      "v_mfma_f32_16x16x32_bf16 v[36:39], a[124:127], a[60:63], v[36:39]\n"
      "ds_read_b128 a[136:139], v1 offset:9216\n"
      "ds_read_b128 a[140:143], v2 offset:9216\n"
      "v_mfma_f32_16x16x32_bf16 v[128:131], a[160:163], v[40:43], 0\n"
      "ds_read_b128 a[144:147], v1 offset:12288\n"
      "ds_read_b128 a[148:151], v2 offset:12288\n"
      "v_mfma_f32_16x16x32_bf16 v[132:135], a[164:167], v[40:43], 0\n"
      "v_max3_f32 v94, v32, v33, v34\n"
      "s_nop 0\n"
      "v_max3_f32 v94, v94, v35, v36\n"
      "v_max3_f32 v94, v94, v37, v38\n"
      "v_max_f32_e32 v94, v94, v39\n"
      "v_mfma_f32_16x16x32_bf16 v[136:139], a[168:171], v[40:43], 0\n"
      "v_mov_b32_e32 v91, v94\n"
      "s_nop 1\n"
      "v_permlane16_swap_b32_e32 v91, v94\n"
      "v_max_f32_e32 v94, v91, v94\n"
      "v_mov_b32_e32 v91, v94\n"
      "v_mfma_f32_16x16x32_bf16 v[140:143], a[172:175], v[40:43], 0\n"
      "s_nop 0\n"
      "v_permlane32_swap_b32_e32 v91, v94\n"
      "v_max_f32_e32 v94, v91, v94\n"
      "v_xor_b32_e32 v60, 0x80000000, v94\n"
      "v_xor_b32_e32 v61, 0x80000000, v94\n"
      "v_mfma_f32_16x16x32_bf16 v[144:147], a[176:179], v[40:43], 0\n"
      "ds_read_b128 a[152:155], v1 offset:13312\n"
      "v_xor_b32_e32 v62, 0x80000000, v94\n"
      "v_xor_b32_e32 v63, 0x80000000, v94\n"
      "v_sub_f32_e32 v32, v32, v94\n"
      "v_sub_f32_e32 v33, v33, v94\n"
      "v_mfma_f32_16x16x32_bf16 v[148:151], a[180:183], v[40:43], 0\n"
      "ds_read_b128 a[156:159], v2 offset:13312\n"
      "v_sub_f32_e32 v34, v34, v94\n"
      "v_sub_f32_e32 v35, v35, v94\n"
      "v_sub_f32_e32 v36, v36, v94\n"
      "v_sub_f32_e32 v37, v37, v94\n"
      "v_mfma_f32_16x16x32_bf16 v[152:155], a[184:187], v[40:43], 0\n"
      "v_sub_f32_e32 v38, v38, v94\n"
      "v_sub_f32_e32 v39, v39, v94\n"
      "v_mfma_f32_16x16x32_bf16 v[156:159], a[188:191], v[40:43], 0\n"
      "v_exp_f32_e32 v32, v32\n"
      "v_exp_f32_e32 v33, v33\n"
      "s_waitcnt lgkmcnt(2)\n"
      "v_mfma_f32_16x16x32_bf16 v[24:27], a[128:131], a[0:3], v[48:51]\n"
      "v_exp_f32_e32 v34, v34\n"
      "v_add_f32_e32 v16, v32, v16\n"
      "v_add_f32_e32 v16, v33, v16\n"
      "v_cvt_pk_bf16_f32 v44, v32, v33\n"
      "v_mfma_f32_16x16x32_bf16 v[24:27], a[132:135], a[4:7], v[24:27]\n"
      "v_exp_f32_e32 v35, v35\n"
      "v_add_f32_e32 v16, v34, v16\n"
      "v_mfma_f32_16x16x32_bf16 v[24:27], a[136:139], a[8:11], v[24:27]\n"
      "v_exp_f32_e32 v36, v36\n"
      "v_exp_f32_e32 v37, v37\n"
      "v_add_f32_e32 v16, v35, v16\n"
      "v_cvt_pk_bf16_f32 v45, v34, v35\n"
      "v_mfma_f32_16x16x32_bf16 v[24:27], a[140:143], a[12:15], v[24:27]\n"
      "v_exp_f32_e32 v38, v38\n"
      "v_add_f32_e32 v16, v36, v16\n"
      "v_add_f32_e32 v16, v37, v16\n"
      "v_cvt_pk_bf16_f32 v46, v36, v37\n"
      "v_mfma_f32_16x16x32_bf16 v[28:31], a[144:147], a[0:3], v[48:51]\n"
      "v_exp_f32_e32 v39, v39\n"
      "v_add_f32_e32 v16, v38, v16\n"
      "v_mfma_f32_16x16x32_bf16 v[28:31], a[148:151], a[4:7], v[28:31]\n"
      "s_waitcnt vmcnt(8)\n"
      "s_barrier\n"
      "s_add_u32 m0, s40, 16384\n"
      "s_mov_b32 s38, 16384\n"
      "buffer_load_dwordx4 v7, s[20:23], s38 offen nt lds\n"
      "s_add_u32 m0, s40, 17280\n"
      "s_nop 0\n"
      "buffer_load_dwordx4 v7, s[20:23], s38 offen offset:128 nt lds\n"
      "v_add_f32_e32 v16, v39, v16\n"
      "v_cvt_pk_bf16_f32 v47, v38, v39\n"
      "s_waitcnt lgkmcnt(0)\n"
      "v_mfma_f32_16x16x32_bf16 v[28:31], a[152:155], a[8:11], v[28:31]\n"
      "ds_read_b64_tr_b16 a[192:193], v3 offset:8192\n"
      "ds_read_b64_tr_b16 a[194:195], v3 offset:12288\n"
      "v_mfma_f32_16x16x32_bf16 v[28:31], a[156:159], a[12:15], v[28:31]\n"
      "ds_read_b64_tr_b16 a[196:197], v4 offset:8192\n"
      "v_exp_f32_e32 v24, v24\n"
      "v_mfma_f32_16x16x32_bf16 v[160:163], a[160:163], v[44:47], 0\n"
      "ds_read_b64_tr_b16 a[198:199], v4 offset:12288\n"
      "ds_read_b64_tr_b16 a[200:201], v5 offset:8192\n"
      "v_add_f32_e32 v13, v24, v13\n"
      "v_mfma_f32_16x16x32_bf16 v[164:167], a[164:167], v[44:47], 0\n"
      "ds_read_b64_tr_b16 a[202:203], v5 offset:12288\n"
      "v_exp_f32_e32 v25, v25\n"
      "v_mfma_f32_16x16x32_bf16 v[168:171], a[168:171], v[44:47], 0\n"
      "v_add_f32_e32 v13, v25, v13\n"
      "v_cvt_pk_bf16_f32 v40, v24, v25\n"
      "v_mfma_f32_16x16x32_bf16 v[172:175], a[172:175], v[44:47], 0\n"
      "ds_read_b64_tr_b16 a[204:205], v6 offset:8192\n"
      "v_exp_f32_e32 v26, v26\n"
      "v_mfma_f32_16x16x32_bf16 v[176:179], a[176:179], v[44:47], 0\n"
      "ds_read_b64_tr_b16 a[206:207], v6 offset:12288\n"
      "ds_read_b64_tr_b16 a[208:209], v3 offset:9216\n"
      "v_add_f32_e32 v13, v26, v13\n"
      "v_mfma_f32_16x16x32_bf16 v[180:183], a[180:183], v[44:47], 0\n"
      "ds_read_b64_tr_b16 a[210:211], v3 offset:13312\n"
      "v_exp_f32_e32 v27, v27\n"
      "v_mfma_f32_16x16x32_bf16 v[184:187], a[184:187], v[44:47], 0\n"
      "v_add_f32_e32 v13, v27, v13\n"
      "v_cvt_pk_bf16_f32 v41, v26, v27\n"
      "v_mfma_f32_16x16x32_bf16 v[188:191], a[188:191], v[44:47], 0\n"
      "ds_read_b64_tr_b16 a[212:213], v4 offset:9216\n"
      "v_exp_f32_e32 v28, v28\n"
      "v_mfma_f32_16x16x32_bf16 v[32:35], a[128:131], a[16:19], v[52:55]\n"
      "ds_read_b64_tr_b16 a[214:215], v4 offset:13312\n"
      "ds_read_b64_tr_b16 a[216:217], v5 offset:9216\n"
      "v_add_f32_e32 v13, v28, v13\n"
      "v_mfma_f32_16x16x32_bf16 v[32:35], a[132:135], a[20:23], v[32:35]\n"
      "ds_read_b64_tr_b16 a[218:219], v5 offset:13312\n"
      "v_exp_f32_e32 v29, v29\n"
      "v_mfma_f32_16x16x32_bf16 v[32:35], a[136:139], a[24:27], v[32:35]\n"
      "ds_read_b64_tr_b16 a[220:221], v6 offset:9216\n"
      "v_add_f32_e32 v13, v29, v13\n"
      "v_cvt_pk_bf16_f32 v42, v28, v29\n"
      "v_mfma_f32_16x16x32_bf16 v[32:35], a[140:143], a[28:31], v[32:35]\n"
      "ds_read_b64_tr_b16 a[222:223], v6 offset:13312\n"
      "v_exp_f32_e32 v30, v30\n"
      "v_mfma_f32_16x16x32_bf16 v[36:39], a[144:147], a[16:19], v[52:55]\n"
      "v_add_f32_e32 v13, v30, v13\n"
      "v_mfma_f32_16x16x32_bf16 v[36:39], a[148:151], a[20:23], v[36:39]\n"
      "v_exp_f32_e32 v31, v31\n"
      "v_mfma_f32_16x16x32_bf16 v[36:39], a[152:155], a[24:27], v[36:39]\n"
      "v_add_f32_e32 v13, v31, v13\n"
      "v_cvt_pk_bf16_f32 v43, v30, v31\n"
      "v_mfma_f32_16x16x32_bf16 v[36:39], a[156:159], a[28:31], v[36:39]\n"
      "v_exp_f32_e32 v32, v32\n"
      "s_waitcnt lgkmcnt(0)\n"
      "v_mfma_f32_16x16x32_bf16 a[224:227], a[192:195], v[40:43], a[224:227]\n"
      "v_add_f32_e32 v14, v32, v14\n"
      "v_mfma_f32_16x16x32_bf16 a[228:231], a[196:199], v[40:43], a[228:231]\n"
      "v_exp_f32_e32 v33, v33\n"
      "v_mfma_f32_16x16x32_bf16 a[232:235], a[200:203], v[40:43], a[232:235]\n"
      "v_add_f32_e32 v14, v33, v14\n"
      "v_cvt_pk_bf16_f32 v44, v32, v33\n"
      "v_mfma_f32_16x16x32_bf16 a[236:239], a[204:207], v[40:43], a[236:239]\n"
      "v_exp_f32_e32 v34, v34\n"
      "v_mfma_f32_16x16x32_bf16 a[240:243], a[208:211], v[40:43], a[240:243]\n"
      "v_add_f32_e32 v14, v34, v14\n"
      "v_mfma_f32_16x16x32_bf16 a[244:247], a[212:215], v[40:43], a[244:247]\n"
      "v_exp_f32_e32 v35, v35\n"
      "v_mfma_f32_16x16x32_bf16 a[248:251], a[216:219], v[40:43], a[248:251]\n"
      "v_add_f32_e32 v14, v35, v14\n"
      "v_cvt_pk_bf16_f32 v45, v34, v35\n"
      "v_mfma_f32_16x16x32_bf16 a[252:255], a[220:223], v[40:43], a[252:255]\n"
      "v_exp_f32_e32 v36, v36\n"
      "v_mfma_f32_16x16x32_bf16 v[24:27], a[128:131], a[32:35], v[56:59]\n"
      "v_add_f32_e32 v14, v36, v14\n"
      "v_mfma_f32_16x16x32_bf16 v[24:27], a[132:135], a[36:39], v[24:27]\n"
      "v_exp_f32_e32 v37, v37\n"
      "v_mfma_f32_16x16x32_bf16 v[24:27], a[136:139], a[40:43], v[24:27]\n"
      "v_add_f32_e32 v14, v37, v14\n"
      "v_cvt_pk_bf16_f32 v46, v36, v37\n"
      "v_mfma_f32_16x16x32_bf16 v[24:27], a[140:143], a[44:47], v[24:27]\n"
      "v_exp_f32_e32 v38, v38\n"
      "v_mfma_f32_16x16x32_bf16 v[28:31], a[144:147], a[32:35], v[56:59]\n"
      "v_add_f32_e32 v14, v38, v14\n"
      "v_mfma_f32_16x16x32_bf16 v[28:31], a[148:151], a[36:39], v[28:31]\n"
      "v_exp_f32_e32 v39, v39\n"
      "v_mfma_f32_16x16x32_bf16 v[28:31], a[152:155], a[40:43], v[28:31]\n"
      "v_add_f32_e32 v14, v39, v14\n"
      "v_cvt_pk_bf16_f32 v47, v38, v39\n"
      "v_mfma_f32_16x16x32_bf16 v[28:31], a[156:159], a[44:47], v[28:31]\n"
      "v_exp_f32_e32 v24, v24\n"
      "v_mfma_f32_16x16x32_bf16 v[96:99], a[192:195], v[44:47], v[96:99]\n"
      "v_add_f32_e32 v15, v24, v15\n"
      "v_mfma_f32_16x16x32_bf16 v[100:103], a[196:199], v[44:47], v[100:103]\n"
      "v_exp_f32_e32 v25, v25\n"
      "v_mfma_f32_16x16x32_bf16 v[104:107], a[200:203], v[44:47], v[104:107]\n"
      "v_add_f32_e32 v15, v25, v15\n"
      "v_cvt_pk_bf16_f32 v40, v24, v25\n"
      "v_mfma_f32_16x16x32_bf16 v[108:111], a[204:207], v[44:47], v[108:111]\n"
      "v_exp_f32_e32 v26, v26\n"
      "v_mfma_f32_16x16x32_bf16 v[112:115], a[208:211], v[44:47], v[112:115]\n"
      "v_add_f32_e32 v15, v26, v15\n"
      "v_mfma_f32_16x16x32_bf16 v[116:119], a[212:215], v[44:47], v[116:119]\n"
      "v_exp_f32_e32 v27, v27\n"
      "v_mfma_f32_16x16x32_bf16 v[120:123], a[216:219], v[44:47], v[120:123]\n"
      "s_add_u32 m0, s40, 81920\n"
      "s_nop 0\n"
      "buffer_load_dwordx4 v8, s[24:27], s38 offen nt lds\n"
      "v_add_f32_e32 v15, v27, v15\n"
      "v_cvt_pk_bf16_f32 v41, v26, v27\n"
      "v_mfma_f32_16x16x32_bf16 v[124:127], a[220:223], v[44:47], v[124:127]\n"
      "v_exp_f32_e32 v28, v28\n"
      "v_mfma_f32_16x16x32_bf16 v[32:35], a[128:131], a[48:51], v[60:63]\n"
      "v_add_f32_e32 v15, v28, v15\n"
      "v_mfma_f32_16x16x32_bf16 v[32:35], a[132:135], a[52:55], v[32:35]\n"
      "v_exp_f32_e32 v29, v29\n"
      "v_mfma_f32_16x16x32_bf16 v[32:35], a[136:139], a[56:59], v[32:35]\n"
      "v_add_f32_e32 v15, v29, v15\n"
      "v_cvt_pk_bf16_f32 v42, v28, v29\n"
      "v_mfma_f32_16x16x32_bf16 v[32:35], a[140:143], a[60:63], v[32:35]\n"
      "v_exp_f32_e32 v30, v30\n"
      "v_mfma_f32_16x16x32_bf16 v[36:39], a[144:147], a[48:51], v[60:63]\n"
      "ds_read_b128 a[96:99], v1 offset:0\n"
      "ds_read_b128 a[100:103], v2 offset:0\n"
      "s_add_u32 m0, s40, 82816\n"
      "s_nop 0\n"
      "buffer_load_dwordx4 v8, s[24:27], s38 offen offset:128 nt lds\n"
      "s_add_u32 m0, s40, 24576\n"
      "s_mov_b32 s38, 24576\n"
      "buffer_load_dwordx4 v7, s[20:23], s38 offen nt lds\n"
      "s_add_u32 m0, s40, 25472\n"
      "s_nop 0\n"
      "buffer_load_dwordx4 v7, s[20:23], s38 offen offset:128 nt lds\n"
      "v_add_f32_e32 v15, v30, v15\n"
      "v_add_u32_e32 v11, 0x10000, v11\n"
      "v_add_u32_e32 v12, 0x10000, v12\n"
      "s_waitcnt vmcnt(10)\n"
      "ds_read_b128 v[72:75], v11 offset:32768\n"
      "ds_read_b128 v[76:79], v12 offset:32768\n"
      "ds_read_b128 v[80:83], v11 offset:33792\n"
      "ds_read_b128 v[84:87], v12 offset:33792\n"
      "v_mfma_f32_16x16x32_bf16 v[36:39], a[148:151], a[52:55], v[36:39]\n"
      "v_exp_f32_e32 v31, v31\n"
      "v_mfma_f32_16x16x32_bf16 v[36:39], a[152:155], a[56:59], v[36:39]\n"
      "v_add_f32_e32 v15, v31, v15\n"
      "v_cvt_pk_bf16_f32 v43, v30, v31\n"
      "s_waitcnt lgkmcnt(3)\n"
      "v_lshlrev_b32_e32 v20, 16, v72\n"
      "v_and_b32_e32 v21, 0xffff0000, v72\n"
      "v_pk_mul_f32 v[20:21], v[20:21], s[52:53]\n"
      "v_lshlrev_b32_e32 v22, 16, v73\n"
      "v_and_b32_e32 v23, 0xffff0000, v73\n"
      "v_cvt_pk_bf16_f32 v72, v20, v21\n"
      "v_pk_mul_f32 v[22:23], v[22:23], s[52:53]\n"
      "v_lshlrev_b32_e32 v92, 16, v74\n"
      "v_mfma_f32_16x16x32_bf16 v[36:39], a[156:159], a[60:63], v[36:39]\n"
      "v_exp_f32_e32 v32, v32\n"
      "v_and_b32_e32 v93, 0xffff0000, v74\n"
      "v_accvgpr_write_b32 a64, v72\n"
      "v_cvt_pk_bf16_f32 v73, v22, v23\n"
      "v_pk_mul_f32 v[92:93], v[92:93], s[52:53]\n"
      "v_lshlrev_b32_e32 v20, 16, v75\n"
      "v_and_b32_e32 v21, 0xffff0000, v75\n"
      "v_accvgpr_write_b32 a65, v73\n"
      "v_cvt_pk_bf16_f32 v74, v92, v93\n"
      "v_mfma_f32_16x16x32_bf16 v[128:131], a[192:195], v[40:43], v[128:131]\n"
      "ds_read_b128 a[104:107], v1 offset:1024\n"
      "ds_read_b128 a[108:111], v2 offset:1024\n"
      "v_add_f32_e32 v16, v32, v16\n"
      "v_pk_mul_f32 v[20:21], v[20:21], s[52:53]\n"
      "s_waitcnt lgkmcnt(4)\n"
      "v_lshlrev_b32_e32 v22, 16, v76\n"
      "v_and_b32_e32 v23, 0xffff0000, v76\n"
      "v_accvgpr_write_b32 a66, v74\n"
      "v_cvt_pk_bf16_f32 v75, v20, v21\n"
      "v_pk_mul_f32 v[22:23], v[22:23], s[52:53]\n"
      "v_lshlrev_b32_e32 v92, 16, v77\n"
      "v_and_b32_e32 v93, 0xffff0000, v77\n"
      "v_mfma_f32_16x16x32_bf16 v[132:135], a[196:199], v[40:43], v[132:135]\n"
      "v_exp_f32_e32 v33, v33\n"
      "v_accvgpr_write_b32 a67, v75\n"
      "v_cvt_pk_bf16_f32 v76, v22, v23\n"
      "v_pk_mul_f32 v[92:93], v[92:93], s[52:53]\n"
      "v_lshlrev_b32_e32 v20, 16, v78\n"
      "v_and_b32_e32 v21, 0xffff0000, v78\n"
      "v_accvgpr_write_b32 a68, v76\n"
      "v_cvt_pk_bf16_f32 v77, v92, v93\n"
      "v_pk_mul_f32 v[20:21], v[20:21], s[52:53]\n"
      "v_mfma_f32_16x16x32_bf16 v[136:139], a[200:203], v[40:43], v[136:139]\n"
      "v_add_f32_e32 v16, v33, v16\n"
      "v_cvt_pk_bf16_f32 v44, v32, v33\n"
      "v_lshlrev_b32_e32 v22, 16, v79\n"
      "v_and_b32_e32 v23, 0xffff0000, v79\n"
      "v_accvgpr_write_b32 a69, v77\n"
      "v_cvt_pk_bf16_f32 v78, v20, v21\n"
      "v_pk_mul_f32 v[22:23], v[22:23], s[52:53]\n"
      "s_waitcnt lgkmcnt(2)\n"
      "v_lshlrev_b32_e32 v92, 16, v80\n"
      "v_and_b32_e32 v93, 0xffff0000, v80\n"
      "v_accvgpr_write_b32 a70, v78\n"
      "v_mfma_f32_16x16x32_bf16 v[140:143], a[204:207], v[40:43], v[140:143]\n"
      "ds_read_b128 a[112:115], v1 offset:4096\n"
      "v_exp_f32_e32 v34, v34\n"
      "v_cvt_pk_bf16_f32 v79, v22, v23\n"
      "v_pk_mul_f32 v[92:93], v[92:93], s[52:53]\n"
      "v_lshlrev_b32_e32 v20, 16, v81\n"
      "v_and_b32_e32 v21, 0xffff0000, v81\n"
      "v_accvgpr_write_b32 a71, v79\n"
      "v_cvt_pk_bf16_f32 v80, v92, v93\n"
      "v_pk_mul_f32 v[20:21], v[20:21], s[52:53]\n"
      "v_lshlrev_b32_e32 v22, 16, v82\n"
      "v_mfma_f32_16x16x32_bf16 v[144:147], a[208:211], v[40:43], v[144:147]\n"
      "ds_read_b128 a[116:119], v2 offset:4096\n"
      "ds_read_b128 a[120:123], v1 offset:5120\n"
      "v_add_f32_e32 v16, v34, v16\n"
      "v_and_b32_e32 v23, 0xffff0000, v82\n"
      "v_accvgpr_write_b32 a72, v80\n"
      "v_cvt_pk_bf16_f32 v81, v20, v21\n"
      "v_pk_mul_f32 v[22:23], v[22:23], s[52:53]\n"
      "v_lshlrev_b32_e32 v92, 16, v83\n"
      "v_and_b32_e32 v93, 0xffff0000, v83\n"
      "v_accvgpr_write_b32 a73, v81\n"
      "v_cvt_pk_bf16_f32 v82, v22, v23\n"
      "v_mfma_f32_16x16x32_bf16 v[148:151], a[212:215], v[40:43], v[148:151]\n"
      "ds_read_b128 a[124:127], v2 offset:5120\n"
      "v_exp_f32_e32 v35, v35\n"
      "v_pk_mul_f32 v[92:93], v[92:93], s[52:53]\n"
      "v_lshlrev_b32_e32 v20, 16, v84\n"
      "v_and_b32_e32 v21, 0xffff0000, v84\n"
      "v_accvgpr_write_b32 a74, v82\n"
      "v_cvt_pk_bf16_f32 v83, v92, v93\n"
      "v_pk_mul_f32 v[20:21], v[20:21], s[52:53]\n"
      "v_lshlrev_b32_e32 v22, 16, v85\n"
      "v_and_b32_e32 v23, 0xffff0000, v85\n"
      "v_mfma_f32_16x16x32_bf16 v[152:155], a[216:219], v[40:43], v[152:155]\n"
      "v_add_f32_e32 v16, v35, v16\n"
      "v_cvt_pk_bf16_f32 v45, v34, v35\n"
      "v_accvgpr_write_b32 a75, v83\n"
      "v_cvt_pk_bf16_f32 v84, v20, v21\n"
      "v_pk_mul_f32 v[22:23], v[22:23], s[52:53]\n"
      "v_lshlrev_b32_e32 v92, 16, v86\n"
      "v_and_b32_e32 v93, 0xffff0000, v86\n"
      "v_accvgpr_write_b32 a76, v84\n"
      "v_cvt_pk_bf16_f32 v85, v22, v23\n"
      "v_pk_mul_f32 v[92:93], v[92:93], s[52:53]\n"
      "v_mfma_f32_16x16x32_bf16 v[156:159], a[220:223], v[40:43], v[156:159]\n"
      "v_exp_f32_e32 v36, v36\n"
      "v_lshlrev_b32_e32 v20, 16, v87\n"
      "v_and_b32_e32 v21, 0xffff0000, v87\n"
      "v_accvgpr_write_b32 a77, v85\n"
      "v_cvt_pk_bf16_f32 v86, v92, v93\n"
      "v_pk_mul_f32 v[20:21], v[20:21], s[52:53]\n"
      "v_accvgpr_write_b32 a78, v86\n"
      "v_cvt_pk_bf16_f32 v87, v20, v21\n"
      "v_accvgpr_write_b32 a79, v87\n"
      "v_mfma_f32_16x16x32_bf16 v[24:27], a[96:99], a[64:67], 0\n"
      "v_add_f32_e32 v16, v36, v16\n"
      "v_mfma_f32_16x16x32_bf16 v[24:27], a[100:103], a[68:71], v[24:27]\n"
      "ds_read_b64_tr_b16 a[160:161], v3 offset:0\n"
      "v_exp_f32_e32 v37, v37\n"
      "s_waitcnt lgkmcnt(1)\n"
      "v_mfma_f32_16x16x32_bf16 v[24:27], a[104:107], a[72:75], v[24:27]\n"
      "v_add_f32_e32 v16, v37, v16\n"
      "v_cvt_pk_bf16_f32 v46, v36, v37\n"
      "v_mfma_f32_16x16x32_bf16 v[24:27], a[108:111], a[76:79], v[24:27]\n"
      "ds_read_b64_tr_b16 a[162:163], v3 offset:4096\n"
      "v_exp_f32_e32 v38, v38\n"
      "v_mfma_f32_16x16x32_bf16 v[28:31], a[112:115], a[64:67], 0\n"
      "ds_read_b64_tr_b16 a[164:165], v4 offset:0\n"
      "ds_read_b64_tr_b16 a[166:167], v4 offset:4096\n"
      "ds_read_b128 a[128:131], v1 offset:8192\n"
      "s_add_u32 m0, s40, 90112\n"
      "s_nop 0\n"
      "buffer_load_dwordx4 v8, s[24:27], s38 offen nt lds\n"
      "v_add_f32_e32 v16, v38, v16\n"
      "v_mfma_f32_16x16x32_bf16 v[28:31], a[116:119], a[68:71], v[28:31]\n"
      "ds_read_b64_tr_b16 a[168:169], v5 offset:0\n"
      "v_exp_f32_e32 v39, v39\n"
      "v_mfma_f32_16x16x32_bf16 v[28:31], a[120:123], a[72:75], v[28:31]\n"
      "ds_read_b128 a[132:135], v2 offset:8192\n"
      "v_add_f32_e32 v16, v39, v16\n"
      "v_cvt_pk_bf16_f32 v47, v38, v39\n"
      "v_mfma_f32_16x16x32_bf16 v[28:31], a[124:127], a[76:79], v[28:31]\n"
      "ds_read_b64_tr_b16 a[170:171], v5 offset:4096\n"
      "ds_read_b64_tr_b16 a[172:173], v6 offset:0\n"
      "ds_read_b128 a[136:139], v1 offset:9216\n"
      "v_mfma_f32_16x16x32_bf16 v[160:163], a[192:195], v[44:47], v[160:163]\n"
      "ds_read_b64_tr_b16 a[174:175], v6 offset:4096\n"
      "ds_read_b64_tr_b16 a[176:177], v3 offset:1024\n"
      "ds_read_b128 a[140:143], v2 offset:9216\n"
      "v_mfma_f32_16x16x32_bf16 v[164:167], a[196:199], v[44:47], v[164:167]\n"
      "ds_read_b128 a[144:147], v1 offset:12288\n"
      "v_max3_f32 v94, v24, v25, v26\n"
      "v_max3_f32 v94, v94, v27, v28\n"
      "v_max3_f32 v94, v94, v29, v30\n"
      "v_max_f32_e32 v94, v94, v31\n"
      "v_mfma_f32_16x16x32_bf16 v[168:171], a[200:203], v[44:47], v[168:171]\n"
      "ds_read_b128 a[148:151], v2 offset:12288\n"
      "v_mov_b32_e32 v91, v94\n"
      "s_nop 1\n"
      "v_permlane16_swap_b32_e32 v91, v94\n"
      "v_max_f32_e32 v94, v91, v94\n"
      "v_mov_b32_e32 v91, v94\n"
      "v_mfma_f32_16x16x32_bf16 v[172:175], a[204:207], v[44:47], v[172:175]\n"
      "ds_read_b128 a[152:155], v1 offset:13312\n"
      "v_permlane32_swap_b32_e32 v91, v94\n"
      "v_max_f32_e32 v94, v91, v94\n"
      "v_xor_b32_e32 v64, 0x80000000, v94\n"
      "v_xor_b32_e32 v65, 0x80000000, v94\n"
      "v_mfma_f32_16x16x32_bf16 v[176:179], a[208:211], v[44:47], v[176:179]\n"
      "ds_read_b128 a[156:159], v2 offset:13312\n"
      "v_xor_b32_e32 v66, 0x80000000, v94\n"
      "v_xor_b32_e32 v67, 0x80000000, v94\n"
      "v_sub_f32_e32 v24, v24, v94\n"
      "v_sub_f32_e32 v25, v25, v94\n"
      "v_mfma_f32_16x16x32_bf16 v[180:183], a[212:215], v[44:47], v[180:183]\n"
      "ds_read_b64_tr_b16 a[178:179], v3 offset:5120\n"
      "v_sub_f32_e32 v26, v26, v94\n"
      "v_sub_f32_e32 v27, v27, v94\n"
      "v_sub_f32_e32 v28, v28, v94\n"
      "v_sub_f32_e32 v29, v29, v94\n"
      "v_mfma_f32_16x16x32_bf16 v[184:187], a[216:219], v[44:47], v[184:187]\n"
      "ds_read_b64_tr_b16 a[180:181], v4 offset:1024\n"
      "v_sub_f32_e32 v30, v30, v94\n"
      "v_sub_f32_e32 v31, v31, v94\n"
      "v_mfma_f32_16x16x32_bf16 v[188:191], a[220:223], v[44:47], v[188:191]\n"
      "ds_read_b64_tr_b16 a[182:183], v4 offset:5120\n"
      "v_exp_f32_e32 v24, v24\n"
      "v_exp_f32_e32 v25, v25\n"
      "s_waitcnt lgkmcnt(4)\n"
      "v_mfma_f32_16x16x32_bf16 v[32:35], a[128:131], a[64:67], v[64:67]\n"
      "ds_read_b64_tr_b16 a[184:185], v5 offset:1024\n"
      "v_exp_f32_e32 v26, v26\n"
      "v_add_f32_e32 v17, v24, v17\n"
      "v_add_f32_e32 v17, v25, v17\n"
      "v_cvt_pk_bf16_f32 v40, v24, v25\n"
      "v_mfma_f32_16x16x32_bf16 v[32:35], a[132:135], a[68:71], v[32:35]\n"
      "ds_read_b64_tr_b16 a[186:187], v5 offset:5120\n"
      "v_exp_f32_e32 v27, v27\n"
      "v_add_f32_e32 v17, v26, v17\n"
      "v_mfma_f32_16x16x32_bf16 v[32:35], a[136:139], a[72:75], v[32:35]\n"
      "ds_read_b64_tr_b16 a[188:189], v6 offset:1024\n"
      "s_add_u32 m0, s40, 91008\n"
      "s_nop 0\n"
      "buffer_load_dwordx4 v8, s[24:27], s38 offen offset:128 nt lds\n"
      "v_exp_f32_e32 v28, v28\n"
      "v_exp_f32_e32 v29, v29\n"
      "v_add_f32_e32 v17, v27, v17\n"
      "v_cvt_pk_bf16_f32 v41, v26, v27\n"
      "v_mfma_f32_16x16x32_bf16 v[32:35], a[140:143], a[76:79], v[32:35]\n"
      "ds_read_b64_tr_b16 a[190:191], v6 offset:5120\n"
      "v_exp_f32_e32 v30, v30\n"
      "v_add_f32_e32 v17, v28, v17\n"
      "v_add_f32_e32 v17, v29, v17\n"
      "v_cvt_pk_bf16_f32 v42, v28, v29\n"
      "v_mfma_f32_16x16x32_bf16 v[36:39], a[144:147], a[64:67], v[64:67]\n"
      "ds_read_b128 a[96:99], v1 offset:0\n"
      "s_add_u32 m0, s40, 32768\n"
      "s_mov_b32 s38, 32768\n"
      "buffer_load_dwordx4 v7, s[20:23], s38 offen nt lds\n"
      "s_add_u32 m0, s40, 33664\n"
      "s_nop 0\n"
      "buffer_load_dwordx4 v7, s[20:23], s38 offen offset:128 nt lds\n"
      "v_exp_f32_e32 v31, v31\n"
      "v_add_f32_e32 v17, v30, v17\n"
      "s_waitcnt vmcnt(10)\n"
      "ds_read_b128 v[72:75], v11 offset:49152\n"
      "ds_read_b128 v[76:79], v12 offset:49152\n"
      "ds_read_b128 v[80:83], v11 offset:50176\n"
      "ds_read_b128 v[84:87], v12 offset:50176\n"
      "v_mfma_f32_16x16x32_bf16 v[36:39], a[148:151], a[68:71], v[36:39]\n"
      "ds_read_b128 a[100:103], v2 offset:0\n"
      "v_add_f32_e32 v17, v31, v17\n"
      "v_cvt_pk_bf16_f32 v43, v30, v31\n"
      "v_mfma_f32_16x16x32_bf16 v[36:39], a[152:155], a[72:75], v[36:39]\n"
      "ds_read_b64_tr_b16 a[192:193], v3 offset:8192\n"
      "ds_read_b64_tr_b16 a[194:195], v3 offset:12288\n"
      "ds_read_b128 a[104:107], v1 offset:1024\n"
      "s_waitcnt lgkmcnt(7)\n"
      "v_lshlrev_b32_e32 v20, 16, v72\n"
      "v_and_b32_e32 v21, 0xffff0000, v72\n"
      "v_pk_mul_f32 v[20:21], v[20:21], s[52:53]\n"
      "v_lshlrev_b32_e32 v22, 16, v73\n"
      "v_and_b32_e32 v23, 0xffff0000, v73\n"
      "v_cvt_pk_bf16_f32 v72, v20, v21\n"
      "v_pk_mul_f32 v[22:23], v[22:23], s[52:53]\n"
      "v_lshlrev_b32_e32 v92, 16, v74\n"
      "v_mfma_f32_16x16x32_bf16 v[36:39], a[156:159], a[76:79], v[36:39]\n"
      "ds_read_b64_tr_b16 a[196:197], v4 offset:8192\n"
      "v_exp_f32_e32 v32, v32\n"
      "v_and_b32_e32 v93, 0xffff0000, v74\n"
      "v_accvgpr_write_b32 a80, v72\n"
      "v_cvt_pk_bf16_f32 v73, v22, v23\n"
      "v_pk_mul_f32 v[92:93], v[92:93], s[52:53]\n"
      "v_lshlrev_b32_e32 v20, 16, v75\n"
      "v_and_b32_e32 v21, 0xffff0000, v75\n"
      "v_accvgpr_write_b32 a81, v73\n"
      "v_cvt_pk_bf16_f32 v74, v92, v93\n"
      "v_mfma_f32_16x16x32_bf16 v[192:195], a[160:163], v[40:43], 0\n"
      "ds_read_b64_tr_b16 a[198:199], v4 offset:12288\n"
      "ds_read_b64_tr_b16 a[200:201], v5 offset:8192\n"
      "ds_read_b128 a[108:111], v2 offset:1024\n"
      "v_add_f32_e32 v17, v32, v17\n"
      "v_pk_mul_f32 v[20:21], v[20:21], s[52:53]\n"
      "s_waitcnt lgkmcnt(10)\n"
      "v_lshlrev_b32_e32 v22, 16, v76\n"
      "v_and_b32_e32 v23, 0xffff0000, v76\n"
      "v_accvgpr_write_b32 a82, v74\n"
      "v_cvt_pk_bf16_f32 v75, v20, v21\n"
      "v_pk_mul_f32 v[22:23], v[22:23], s[52:53]\n"
      "v_lshlrev_b32_e32 v92, 16, v77\n"
      "v_and_b32_e32 v93, 0xffff0000, v77\n"
      "v_mfma_f32_16x16x32_bf16 v[196:199], a[164:167], v[40:43], 0\n"
      "ds_read_b64_tr_b16 a[202:203], v5 offset:12288\n"
      "v_exp_f32_e32 v33, v33\n"
      "v_accvgpr_write_b32 a83, v75\n"
      "v_cvt_pk_bf16_f32 v76, v22, v23\n"
      "v_pk_mul_f32 v[92:93], v[92:93], s[52:53]\n"
      "v_lshlrev_b32_e32 v20, 16, v78\n"
      "v_and_b32_e32 v21, 0xffff0000, v78\n"
      "v_accvgpr_write_b32 a84, v76\n"
      "v_cvt_pk_bf16_f32 v77, v92, v93\n"
      "v_pk_mul_f32 v[20:21], v[20:21], s[52:53]\n"
      "v_mfma_f32_16x16x32_bf16 v[200:203], a[168:171], v[40:43], 0\n"
      "ds_read_b128 a[112:115], v1 offset:4096\n"
      "v_add_f32_e32 v17, v33, v17\n"
      "v_cvt_pk_bf16_f32 v44, v32, v33\n"
      "v_lshlrev_b32_e32 v22, 16, v79\n"
      "v_and_b32_e32 v23, 0xffff0000, v79\n"
      "v_accvgpr_write_b32 a85, v77\n"
      "v_cvt_pk_bf16_f32 v78, v20, v21\n"
      "v_pk_mul_f32 v[22:23], v[22:23], s[52:53]\n"
      "s_waitcnt lgkmcnt(9)\n"
      "v_lshlrev_b32_e32 v92, 16, v80\n"
      "v_and_b32_e32 v93, 0xffff0000, v80\n"
      "v_accvgpr_write_b32 a86, v78\n"
      "v_mfma_f32_16x16x32_bf16 v[204:207], a[172:175], v[40:43], 0\n"
      "ds_read_b64_tr_b16 a[204:205], v6 offset:8192\n"
      "ds_read_b128 a[116:119], v2 offset:4096\n"
      "v_exp_f32_e32 v34, v34\n"
      "v_cvt_pk_bf16_f32 v79, v22, v23\n"
      "v_pk_mul_f32 v[92:93], v[92:93], s[52:53]\n"
      "v_lshlrev_b32_e32 v20, 16, v81\n"
      "v_and_b32_e32 v21, 0xffff0000, v81\n"
      "v_accvgpr_write_b32 a87, v79\n"
      "v_cvt_pk_bf16_f32 v80, v92, v93\n"
      "v_pk_mul_f32 v[20:21], v[20:21], s[52:53]\n"
      "v_lshlrev_b32_e32 v22, 16, v82\n"
      "v_mfma_f32_16x16x32_bf16 v[208:211], a[176:179], v[40:43], 0\n"
      "ds_read_b64_tr_b16 a[206:207], v6 offset:12288\n"
      "ds_read_b64_tr_b16 a[208:209], v3 offset:9216\n"
      "ds_read_b128 a[120:123], v1 offset:5120\n"
      "v_add_f32_e32 v17, v34, v17\n"
      "v_and_b32_e32 v23, 0xffff0000, v82\n"
      "v_accvgpr_write_b32 a88, v80\n"
      "v_cvt_pk_bf16_f32 v81, v20, v21\n"
      "v_pk_mul_f32 v[22:23], v[22:23], s[52:53]\n"
      "v_lshlrev_b32_e32 v92, 16, v83\n"
      "v_and_b32_e32 v93, 0xffff0000, v83\n"
      "v_accvgpr_write_b32 a89, v81\n"
      "v_cvt_pk_bf16_f32 v82, v22, v23\n"
      "v_mfma_f32_16x16x32_bf16 v[212:215], a[180:183], v[40:43], 0\n"
      "ds_read_b64_tr_b16 a[210:211], v3 offset:13312\n"
      "ds_read_b128 a[124:127], v2 offset:5120\n"
      "v_exp_f32_e32 v35, v35\n"
      "v_pk_mul_f32 v[92:93], v[92:93], s[52:53]\n"
      "v_lshlrev_b32_e32 v20, 16, v84\n"
      "v_and_b32_e32 v21, 0xffff0000, v84\n"
      "v_accvgpr_write_b32 a90, v82\n"
      "v_cvt_pk_bf16_f32 v83, v92, v93\n"
      "v_pk_mul_f32 v[20:21], v[20:21], s[52:53]\n"
      "v_lshlrev_b32_e32 v22, 16, v85\n"
      "v_and_b32_e32 v23, 0xffff0000, v85\n"
      "v_mfma_f32_16x16x32_bf16 v[216:219], a[184:187], v[40:43], 0\n"
      "v_add_f32_e32 v17, v35, v17\n"
      "v_cvt_pk_bf16_f32 v45, v34, v35\n"
      "v_accvgpr_write_b32 a91, v83\n"
      "v_cvt_pk_bf16_f32 v84, v20, v21\n"
      "v_pk_mul_f32 v[22:23], v[22:23], s[52:53]\n"
      "v_lshlrev_b32_e32 v92, 16, v86\n"
      "v_and_b32_e32 v93, 0xffff0000, v86\n"
      "v_accvgpr_write_b32 a92, v84\n"
      "v_cvt_pk_bf16_f32 v85, v22, v23\n"
      "v_pk_mul_f32 v[92:93], v[92:93], s[52:53]\n"
      "v_mfma_f32_16x16x32_bf16 v[220:223], a[188:191], v[40:43], 0\n"
      "ds_read_b64_tr_b16 a[212:213], v4 offset:9216\n"
      "v_exp_f32_e32 v36, v36\n"
      "v_lshlrev_b32_e32 v20, 16, v87\n"
      "v_and_b32_e32 v21, 0xffff0000, v87\n"
      "v_accvgpr_write_b32 a93, v85\n"
      "v_cvt_pk_bf16_f32 v86, v92, v93\n"
      "v_pk_mul_f32 v[20:21], v[20:21], s[52:53]\n"
      "v_accvgpr_write_b32 a94, v86\n"
      "v_cvt_pk_bf16_f32 v87, v20, v21\n"
      "v_accvgpr_write_b32 a95, v87\n"
      "v_mfma_f32_16x16x32_bf16 v[24:27], a[96:99], a[80:83], 0\n"
      "ds_read_b64_tr_b16 a[214:215], v4 offset:13312\n"
      "ds_read_b64_tr_b16 a[216:217], v5 offset:9216\n"
      "v_add_f32_e32 v17, v36, v17\n"
      "v_mfma_f32_16x16x32_bf16 v[24:27], a[100:103], a[84:87], v[24:27]\n"
      "ds_read_b64_tr_b16 a[218:219], v5 offset:13312\n"
      "v_exp_f32_e32 v37, v37\n"
      "s_waitcnt lgkmcnt(4)\n"
      "v_mfma_f32_16x16x32_bf16 v[24:27], a[104:107], a[88:91], v[24:27]\n"
      "ds_read_b64_tr_b16 a[220:221], v6 offset:9216\n"
      "v_add_f32_e32 v17, v37, v17\n"
      "v_cvt_pk_bf16_f32 v46, v36, v37\n"
      "v_mfma_f32_16x16x32_bf16 v[24:27], a[108:111], a[92:95], v[24:27]\n"
      "ds_read_b64_tr_b16 a[222:223], v6 offset:13312\n"
      "v_exp_f32_e32 v38, v38\n"
      "v_mfma_f32_16x16x32_bf16 v[28:31], a[112:115], a[80:83], 0\n"
      "ds_read_b64_tr_b16 a[160:161], v3 offset:0\n"
      "ds_read_b64_tr_b16 a[162:163], v3 offset:4096\n"
      "ds_read_b128 a[128:131], v1 offset:8192\n"
      "s_add_u32 m0, s40, 98304\n"
      "s_barrier\n"
      "buffer_load_dwordx4 v8, s[24:27], s38 offen nt lds\n"
      "v_add_f32_e32 v17, v38, v17\n"
      "v_mfma_f32_16x16x32_bf16 v[28:31], a[116:119], a[84:87], v[28:31]\n"
      "ds_read_b64_tr_b16 a[164:165], v4 offset:0\n"
      "ds_read_b128 a[132:135], v2 offset:8192\n"
      "v_exp_f32_e32 v39, v39\n"
      "v_mfma_f32_16x16x32_bf16 v[28:31], a[120:123], a[88:91], v[28:31]\n"
      "ds_read_b128 a[136:139], v1 offset:9216\n"
      "v_add_f32_e32 v17, v39, v17\n"
      "v_cvt_pk_bf16_f32 v47, v38, v39\n"
      "v_mfma_f32_16x16x32_bf16 v[28:31], a[124:127], a[92:95], v[28:31]\n"
      "ds_read_b64_tr_b16 a[166:167], v4 offset:4096\n"
      "ds_read_b64_tr_b16 a[168:169], v5 offset:0\n"
      "ds_read_b128 a[140:143], v2 offset:9216\n"
      "v_mfma_f32_16x16x32_bf16 v[192:195], a[192:195], v[44:47], v[192:195]\n"
      "ds_read_b64_tr_b16 a[170:171], v5 offset:4096\n"
      "ds_read_b64_tr_b16 a[172:173], v6 offset:0\n"
      "ds_read_b128 a[144:147], v1 offset:12288\n"
      "v_mfma_f32_16x16x32_bf16 v[196:199], a[196:199], v[44:47], v[196:199]\n"
      "ds_read_b128 a[148:151], v2 offset:12288\n"
      "v_max3_f32 v94, v24, v25, v26\n"
      "v_max3_f32 v94, v94, v27, v28\n"
      "v_max3_f32 v94, v94, v29, v30\n"
      "v_max_f32_e32 v94, v94, v31\n"
      "v_mfma_f32_16x16x32_bf16 v[200:203], a[200:203], v[44:47], v[200:203]\n"
      "ds_read_b128 a[152:155], v1 offset:13312\n"
      "v_mov_b32_e32 v91, v94\n"
      "s_nop 1\n"
      "v_permlane16_swap_b32_e32 v91, v94\n"
      "v_max_f32_e32 v94, v91, v94\n"
      "v_mov_b32_e32 v91, v94\n"
      "v_mfma_f32_16x16x32_bf16 v[204:207], a[204:207], v[44:47], v[204:207]\n"
      "ds_read_b64_tr_b16 a[174:175], v6 offset:4096\n"
      "v_permlane32_swap_b32_e32 v91, v94\n"
      "v_max_f32_e32 v94, v91, v94\n"
      "v_xor_b32_e32 v68, 0x80000000, v94\n"
      "v_xor_b32_e32 v69, 0x80000000, v94\n"
      "v_mfma_f32_16x16x32_bf16 v[208:211], a[208:211], v[44:47], v[208:211]\n"
      "ds_read_b64_tr_b16 a[176:177], v3 offset:1024\n"
      "v_xor_b32_e32 v70, 0x80000000, v94\n"
      "v_xor_b32_e32 v71, 0x80000000, v94\n"
      "v_sub_f32_e32 v24, v24, v94\n"
      "v_sub_f32_e32 v25, v25, v94\n"
      "s_waitcnt lgkmcnt(4)\n"
      "v_mfma_f32_16x16x32_bf16 v[212:215], a[212:215], v[44:47], v[212:215]\n"
      "ds_read_b64_tr_b16 a[178:179], v3 offset:5120\n"
      "ds_read_b128 a[156:159], v2 offset:13312\n"
      "v_sub_f32_e32 v26, v26, v94\n"
      "v_sub_f32_e32 v27, v27, v94\n"
      "v_sub_f32_e32 v28, v28, v94\n"
      "v_sub_f32_e32 v29, v29, v94\n"
      "v_mfma_f32_16x16x32_bf16 v[216:219], a[216:219], v[44:47], v[216:219]\n"
      "ds_read_b64_tr_b16 a[180:181], v4 offset:1024\n"
      "v_sub_f32_e32 v30, v30, v94\n"
      "v_sub_f32_e32 v31, v31, v94\n"
      "v_mfma_f32_16x16x32_bf16 v[220:223], a[220:223], v[44:47], v[220:223]\n"
      "ds_read_b64_tr_b16 a[182:183], v4 offset:5120\n"
      "v_exp_f32_e32 v24, v24\n"
      "v_exp_f32_e32 v25, v25\n"
      "v_mfma_f32_16x16x32_bf16 v[32:35], a[128:131], a[80:83], v[68:71]\n"
      "ds_read_b64_tr_b16 a[184:185], v5 offset:1024\n"
      "s_add_u32 m0, s40, 99200\n"
      "s_nop 0\n"
      "buffer_load_dwordx4 v8, s[24:27], s38 offen offset:128 nt lds\n"
      "v_exp_f32_e32 v26, v26\n"
      "v_add_f32_e32 v18, v24, v18\n"
      "v_add_f32_e32 v18, v25, v18\n"
      "v_cvt_pk_bf16_f32 v40, v24, v25\n"
      "v_mfma_f32_16x16x32_bf16 v[32:35], a[132:135], a[84:87], v[32:35]\n"
      "ds_read_b64_tr_b16 a[186:187], v5 offset:5120\n"
      "v_exp_f32_e32 v27, v27\n"
      "v_add_f32_e32 v18, v26, v18\n"
      "v_mfma_f32_16x16x32_bf16 v[32:35], a[136:139], a[88:91], v[32:35]\n"
      "ds_read_b64_tr_b16 a[188:189], v6 offset:1024\n"
      "v_exp_f32_e32 v28, v28\n"
      "v_exp_f32_e32 v29, v29\n"
      "v_add_f32_e32 v18, v27, v18\n"
      "v_cvt_pk_bf16_f32 v41, v26, v27\n"
      "v_mfma_f32_16x16x32_bf16 v[32:35], a[140:143], a[92:95], v[32:35]\n"
      "ds_read_b64_tr_b16 a[190:191], v6 offset:5120\n"
      "s_waitcnt vmcnt(10)\n"
      "s_barrier\n"
      "v_exp_f32_e32 v30, v30\n"
      "v_add_f32_e32 v18, v28, v18\n"
      "v_add_f32_e32 v18, v29, v18\n"
      "v_cvt_pk_bf16_f32 v42, v28, v29\n"
      "v_mfma_f32_16x16x32_bf16 v[36:39], a[144:147], a[80:83], v[68:71]\n"
      "ds_read_b128 a[96:99], v1 offset:16384\n"
      "v_exp_f32_e32 v31, v31\n"
      "v_add_f32_e32 v18, v30, v18\n"
      "s_waitcnt lgkmcnt(4)\n"
      "v_mfma_f32_16x16x32_bf16 v[36:39], a[148:151], a[84:87], v[36:39]\n"
      "ds_read_b128 a[100:103], v2 offset:16384\n"
      "v_add_f32_e32 v18, v31, v18\n"
      "v_cvt_pk_bf16_f32 v43, v30, v31\n"
      "v_mfma_f32_16x16x32_bf16 v[36:39], a[152:155], a[88:91], v[36:39]\n"
      "ds_read_b64_tr_b16 a[192:193], v3 offset:8192\n"
      "ds_read_b64_tr_b16 a[194:195], v3 offset:12288\n"
      "ds_read_b128 a[104:107], v1 offset:17408\n"
      "v_mfma_f32_16x16x32_bf16 v[36:39], a[156:159], a[92:95], v[36:39]\n"
      "ds_read_b64_tr_b16 a[196:197], v4 offset:8192\n"
      "v_exp_f32_e32 v32, v32\n"
      "v_mfma_f32_16x16x32_bf16 v[224:227], a[160:163], v[40:43], 0\n"
      "ds_read_b64_tr_b16 a[198:199], v4 offset:12288\n"
      "ds_read_b64_tr_b16 a[200:201], v5 offset:8192\n"
      "ds_read_b128 a[108:111], v2 offset:17408\n"
      "v_add_f32_e32 v18, v32, v18\n"
      "v_mfma_f32_16x16x32_bf16 v[228:231], a[164:167], v[40:43], 0\n"
      "ds_read_b64_tr_b16 a[202:203], v5 offset:12288\n"
      "v_exp_f32_e32 v33, v33\n"
      "v_mfma_f32_16x16x32_bf16 v[232:235], a[168:171], v[40:43], 0\n"
      "ds_read_b128 a[112:115], v1 offset:20480\n"
      "v_add_f32_e32 v18, v33, v18\n"
      "v_cvt_pk_bf16_f32 v44, v32, v33\n"
      "v_mfma_f32_16x16x32_bf16 v[236:239], a[172:175], v[40:43], 0\n"
      "ds_read_b64_tr_b16 a[204:205], v6 offset:8192\n"
      "ds_read_b128 a[116:119], v2 offset:20480\n"
      "v_exp_f32_e32 v34, v34\n"
      "v_mfma_f32_16x16x32_bf16 v[240:243], a[176:179], v[40:43], 0\n"
      "ds_read_b64_tr_b16 a[206:207], v6 offset:12288\n"
      "ds_read_b64_tr_b16 a[208:209], v3 offset:9216\n"
      "ds_read_b128 a[120:123], v1 offset:21504\n"
      "v_add_f32_e32 v18, v34, v18\n"
      "v_mfma_f32_16x16x32_bf16 v[244:247], a[180:183], v[40:43], 0\n"
      "ds_read_b64_tr_b16 a[210:211], v3 offset:13312\n"
      "ds_read_b128 a[124:127], v2 offset:21504\n"
      "v_exp_f32_e32 v35, v35\n"
      "s_waitcnt lgkmcnt(8)\n"
      "v_mfma_f32_16x16x32_bf16 v[248:251], a[184:187], v[40:43], 0\n"
      "s_add_u32 m0, s40, 40960\n"
      "s_mov_b32 s38, 40960\n"
      "buffer_load_dwordx4 v7, s[20:23], s38 offen nt lds\n"
      "v_add_f32_e32 v18, v35, v18\n"
      "v_cvt_pk_bf16_f32 v45, v34, v35\n"
      "v_mfma_f32_16x16x32_bf16 v[252:255], a[188:191], v[40:43], 0\n"
      "ds_read_b64_tr_b16 a[212:213], v4 offset:9216\n"
      "v_exp_f32_e32 v36, v36\n"
      "v_mfma_f32_16x16x32_bf16 v[24:27], a[96:99], a[0:3], v[48:51]\n"
      "ds_read_b64_tr_b16 a[214:215], v4 offset:13312\n"
      "ds_read_b64_tr_b16 a[216:217], v5 offset:9216\n"
      "v_add_f32_e32 v18, v36, v18\n"
      "v_mfma_f32_16x16x32_bf16 v[24:27], a[100:103], a[4:7], v[24:27]\n"
      "ds_read_b64_tr_b16 a[218:219], v5 offset:13312\n"
      "v_exp_f32_e32 v37, v37\n"
      "v_mfma_f32_16x16x32_bf16 v[24:27], a[104:107], a[8:11], v[24:27]\n"
      "ds_read_b64_tr_b16 a[220:221], v6 offset:9216\n"
      "v_add_f32_e32 v18, v37, v18\n"
      "v_cvt_pk_bf16_f32 v46, v36, v37\n"
      "v_mfma_f32_16x16x32_bf16 v[24:27], a[108:111], a[12:15], v[24:27]\n"
      "ds_read_b64_tr_b16 a[222:223], v6 offset:13312\n"
      "s_waitcnt vmcnt(9)\n"
      "s_barrier\n"
      "s_add_u32 m0, s40, 41856\n"
      "s_nop 0\n"
      "buffer_load_dwordx4 v7, s[20:23], s38 offen offset:128 nt lds\n"
      "v_exp_f32_e32 v38, v38\n"
      "s_waitcnt lgkmcnt(5)\n"
      "v_mfma_f32_16x16x32_bf16 v[28:31], a[112:115], a[0:3], v[48:51]\n"
      "ds_read_b64_tr_b16 a[160:161], v3 offset:16384\n"
      "ds_read_b64_tr_b16 a[162:163], v3 offset:20480\n"
      "v_add_f32_e32 v18, v38, v18\n"
      "v_mfma_f32_16x16x32_bf16 v[28:31], a[116:119], a[4:7], v[28:31]\n"
      "ds_read_b64_tr_b16 a[164:165], v4 offset:16384\n"
      "v_exp_f32_e32 v39, v39\n"
      "v_mfma_f32_16x16x32_bf16 v[28:31], a[120:123], a[8:11], v[28:31]\n"
      "v_add_f32_e32 v18, v39, v18\n"
      "v_cvt_pk_bf16_f32 v47, v38, v39\n"
      "v_mfma_f32_16x16x32_bf16 v[28:31], a[124:127], a[12:15], v[28:31]\n"
      "ds_read_b64_tr_b16 a[166:167], v4 offset:20480\n"
      "v_exp_f32_e32 v24, v24\n"
      "v_mfma_f32_16x16x32_bf16 v[224:227], a[192:195], v[44:47], v[224:227]\n"
      "ds_read_b64_tr_b16 a[168:169], v5 offset:16384\n"
      "ds_read_b64_tr_b16 a[170:171], v5 offset:20480\n"
      "v_add_f32_e32 v13, v24, v13\n"
      "v_mfma_f32_16x16x32_bf16 v[228:231], a[196:199], v[44:47], v[228:231]\n"
      "ds_read_b64_tr_b16 a[172:173], v6 offset:16384\n"
      "v_exp_f32_e32 v25, v25\n"
      "v_mfma_f32_16x16x32_bf16 v[232:235], a[200:203], v[44:47], v[232:235]\n"
      "v_add_f32_e32 v13, v25, v13\n"
      "v_cvt_pk_bf16_f32 v40, v24, v25\n"
      "v_mfma_f32_16x16x32_bf16 v[236:239], a[204:207], v[44:47], v[236:239]\n"
      "ds_read_b64_tr_b16 a[174:175], v6 offset:20480\n"
      "v_exp_f32_e32 v26, v26\n"
      "v_mfma_f32_16x16x32_bf16 v[240:243], a[208:211], v[44:47], v[240:243]\n"
      "ds_read_b64_tr_b16 a[176:177], v3 offset:17408\n"
      "ds_read_b64_tr_b16 a[178:179], v3 offset:21504\n"
      "v_add_f32_e32 v13, v26, v13\n"
      "s_waitcnt lgkmcnt(4)\n"
      "v_mfma_f32_16x16x32_bf16 v[244:247], a[212:215], v[44:47], v[244:247]\n"
      "ds_read_b64_tr_b16 a[180:181], v4 offset:17408\n"
      "v_exp_f32_e32 v27, v27\n"
      "v_mfma_f32_16x16x32_bf16 v[248:251], a[216:219], v[44:47], v[248:251]\n"
      "v_add_f32_e32 v13, v27, v13\n"
      "v_cvt_pk_bf16_f32 v41, v26, v27\n"
      "v_mfma_f32_16x16x32_bf16 v[252:255], a[220:223], v[44:47], v[252:255]\n"
      "ds_read_b64_tr_b16 a[182:183], v4 offset:21504\n"
      "v_exp_f32_e32 v28, v28\n"
      "v_mfma_f32_16x16x32_bf16 v[32:35], a[96:99], a[16:19], v[52:55]\n"
      "ds_read_b64_tr_b16 a[184:185], v5 offset:17408\n"
      "ds_read_b64_tr_b16 a[186:187], v5 offset:21504\n"
      "v_add_f32_e32 v13, v28, v13\n"
      "v_mfma_f32_16x16x32_bf16 v[32:35], a[100:103], a[20:23], v[32:35]\n"
      "ds_read_b64_tr_b16 a[188:189], v6 offset:17408\n"
      "v_exp_f32_e32 v29, v29\n"
      "v_mfma_f32_16x16x32_bf16 v[32:35], a[104:107], a[24:27], v[32:35]\n"
      "v_add_f32_e32 v13, v29, v13\n"
      "v_cvt_pk_bf16_f32 v42, v28, v29\n"
      "v_mfma_f32_16x16x32_bf16 v[32:35], a[108:111], a[28:31], v[32:35]\n"
      "ds_read_b64_tr_b16 a[190:191], v6 offset:21504\n"
      "v_exp_f32_e32 v30, v30\n"
      "v_mfma_f32_16x16x32_bf16 v[36:39], a[112:115], a[16:19], v[52:55]\n"
      "v_add_f32_e32 v13, v30, v13\n"
      "v_mfma_f32_16x16x32_bf16 v[36:39], a[116:119], a[20:23], v[36:39]\n"
      "v_exp_f32_e32 v31, v31\n"
      "v_mfma_f32_16x16x32_bf16 v[36:39], a[120:123], a[24:27], v[36:39]\n"
      "v_add_f32_e32 v13, v31, v13\n"
      "v_cvt_pk_bf16_f32 v43, v30, v31\n"
      "v_mfma_f32_16x16x32_bf16 v[36:39], a[124:127], a[28:31], v[36:39]\n"
      "v_exp_f32_e32 v32, v32\n"
      "v_mfma_f32_16x16x32_bf16 a[224:227], a[160:163], v[40:43], a[224:227]\n"
      "v_add_f32_e32 v14, v32, v14\n"
      "v_mfma_f32_16x16x32_bf16 a[228:231], a[164:167], v[40:43], a[228:231]\n"
      "v_exp_f32_e32 v33, v33\n"
      "v_mfma_f32_16x16x32_bf16 a[232:235], a[168:171], v[40:43], a[232:235]\n"
      "v_add_f32_e32 v14, v33, v14\n"
      "v_cvt_pk_bf16_f32 v44, v32, v33\n"
      "s_waitcnt lgkmcnt(0)\n"
      "v_mfma_f32_16x16x32_bf16 a[236:239], a[172:175], v[40:43], a[236:239]\n"
      "v_exp_f32_e32 v34, v34\n"
      "v_mfma_f32_16x16x32_bf16 a[240:243], a[176:179], v[40:43], a[240:243]\n"
      "s_add_u32 m0, s40, 106496\n"
      "s_nop 0\n"
      "buffer_load_dwordx4 v8, s[24:27], s38 offen nt lds\n"
      "v_add_f32_e32 v14, v34, v14\n"
      "v_mfma_f32_16x16x32_bf16 a[244:247], a[180:183], v[40:43], a[244:247]\n"
      "v_exp_f32_e32 v35, v35\n"
      "v_mfma_f32_16x16x32_bf16 a[248:251], a[184:187], v[40:43], a[248:251]\n"
      "v_add_f32_e32 v14, v35, v14\n"
      "v_cvt_pk_bf16_f32 v45, v34, v35\n"
      "v_mfma_f32_16x16x32_bf16 a[252:255], a[188:191], v[40:43], a[252:255]\n"
      "v_exp_f32_e32 v36, v36\n"
      "v_mfma_f32_16x16x32_bf16 v[24:27], a[96:99], a[32:35], v[56:59]\n"
      "v_add_f32_e32 v14, v36, v14\n"
      "v_mfma_f32_16x16x32_bf16 v[24:27], a[100:103], a[36:39], v[24:27]\n"
      "v_exp_f32_e32 v37, v37\n"
      "v_mfma_f32_16x16x32_bf16 v[24:27], a[104:107], a[40:43], v[24:27]\n"
      "v_add_f32_e32 v14, v37, v14\n"
      "v_cvt_pk_bf16_f32 v46, v36, v37\n"
      "v_mfma_f32_16x16x32_bf16 v[24:27], a[108:111], a[44:47], v[24:27]\n"
      "v_exp_f32_e32 v38, v38\n"
      "v_mfma_f32_16x16x32_bf16 v[28:31], a[112:115], a[32:35], v[56:59]\n"
      "v_add_f32_e32 v14, v38, v14\n"
      "v_mfma_f32_16x16x32_bf16 v[28:31], a[116:119], a[36:39], v[28:31]\n"
      "v_exp_f32_e32 v39, v39\n"
      "v_mfma_f32_16x16x32_bf16 v[28:31], a[120:123], a[40:43], v[28:31]\n"
      "v_add_f32_e32 v14, v39, v14\n"
      "v_cvt_pk_bf16_f32 v47, v38, v39\n"
      "v_mfma_f32_16x16x32_bf16 v[28:31], a[124:127], a[44:47], v[28:31]\n"
      "v_exp_f32_e32 v24, v24\n"
      "v_mfma_f32_16x16x32_bf16 v[96:99], a[160:163], v[44:47], v[96:99]\n"
      "v_add_f32_e32 v15, v24, v15\n"
      "v_mfma_f32_16x16x32_bf16 v[100:103], a[164:167], v[44:47], v[100:103]\n"
      "v_exp_f32_e32 v25, v25\n"
      "v_mfma_f32_16x16x32_bf16 v[104:107], a[168:171], v[44:47], v[104:107]\n"
      "v_add_f32_e32 v15, v25, v15\n"
      "v_cvt_pk_bf16_f32 v40, v24, v25\n"
      "v_mfma_f32_16x16x32_bf16 v[108:111], a[172:175], v[44:47], v[108:111]\n"
      "v_exp_f32_e32 v26, v26\n"
      "v_mfma_f32_16x16x32_bf16 v[112:115], a[176:179], v[44:47], v[112:115]\n"
      "s_add_u32 m0, s40, 107392\n"
      "s_nop 0\n"
      "buffer_load_dwordx4 v8, s[24:27], s38 offen offset:128 nt lds\n"
      "v_add_f32_e32 v15, v26, v15\n"
      "v_mfma_f32_16x16x32_bf16 v[116:119], a[180:183], v[44:47], v[116:119]\n"
      "v_exp_f32_e32 v27, v27\n"
      "v_mfma_f32_16x16x32_bf16 v[120:123], a[184:187], v[44:47], v[120:123]\n"
      "v_add_f32_e32 v15, v27, v15\n"
      "v_cvt_pk_bf16_f32 v41, v26, v27\n"
      "v_mfma_f32_16x16x32_bf16 v[124:127], a[188:191], v[44:47], v[124:127]\n"
      "v_exp_f32_e32 v28, v28\n"
      "v_mfma_f32_16x16x32_bf16 v[32:35], a[96:99], a[48:51], v[60:63]\n"
      "v_add_f32_e32 v15, v28, v15\n"
      "v_mfma_f32_16x16x32_bf16 v[32:35], a[100:103], a[52:55], v[32:35]\n"
      "v_exp_f32_e32 v29, v29\n"
      "v_mfma_f32_16x16x32_bf16 v[32:35], a[104:107], a[56:59], v[32:35]\n"
      "v_add_f32_e32 v15, v29, v15\n"
      "v_cvt_pk_bf16_f32 v42, v28, v29\n"
      "v_mfma_f32_16x16x32_bf16 v[32:35], a[108:111], a[60:63], v[32:35]\n"
      "v_exp_f32_e32 v30, v30\n"
      "v_mfma_f32_16x16x32_bf16 v[36:39], a[112:115], a[48:51], v[60:63]\n"
      "v_add_f32_e32 v15, v30, v15\n"
      "v_mfma_f32_16x16x32_bf16 v[36:39], a[116:119], a[52:55], v[36:39]\n"
      "v_exp_f32_e32 v31, v31\n"
      "v_mfma_f32_16x16x32_bf16 v[36:39], a[120:123], a[56:59], v[36:39]\n"
      "v_add_f32_e32 v15, v31, v15\n"
      "v_cvt_pk_bf16_f32 v43, v30, v31\n"
      "v_mfma_f32_16x16x32_bf16 v[36:39], a[124:127], a[60:63], v[36:39]\n"
      "v_exp_f32_e32 v32, v32\n"
      "v_mfma_f32_16x16x32_bf16 v[128:131], a[160:163], v[40:43], v[128:131]\n"
      "v_add_f32_e32 v16, v32, v16\n"
      "v_mfma_f32_16x16x32_bf16 v[132:135], a[164:167], v[40:43], v[132:135]\n"
      "v_exp_f32_e32 v33, v33\n"
      "v_mfma_f32_16x16x32_bf16 v[136:139], a[168:171], v[40:43], v[136:139]\n"
      "s_add_u32 m0, s40, 49152\n"
      "s_mov_b32 s38, 49152\n"
      "buffer_load_dwordx4 v7, s[20:23], s38 offen nt lds\n"
      "v_add_f32_e32 v16, v33, v16\n"
      "v_cvt_pk_bf16_f32 v44, v32, v33\n"
      "v_mfma_f32_16x16x32_bf16 v[140:143], a[172:175], v[40:43], v[140:143]\n"
      "v_exp_f32_e32 v34, v34\n"
      "v_mfma_f32_16x16x32_bf16 v[144:147], a[176:179], v[40:43], v[144:147]\n"
      "v_add_f32_e32 v16, v34, v16\n"
      "v_mfma_f32_16x16x32_bf16 v[148:151], a[180:183], v[40:43], v[148:151]\n"
      "v_exp_f32_e32 v35, v35\n"
      "v_mfma_f32_16x16x32_bf16 v[152:155], a[184:187], v[40:43], v[152:155]\n"
      "v_add_f32_e32 v16, v35, v16\n"
      "v_cvt_pk_bf16_f32 v45, v34, v35\n"
      "v_mfma_f32_16x16x32_bf16 v[156:159], a[188:191], v[40:43], v[156:159]\n"
      "v_exp_f32_e32 v36, v36\n"
      "v_mfma_f32_16x16x32_bf16 v[24:27], a[96:99], a[64:67], v[64:67]\n"
      "v_add_f32_e32 v16, v36, v16\n"
      "v_mfma_f32_16x16x32_bf16 v[24:27], a[100:103], a[68:71], v[24:27]\n"
      "v_exp_f32_e32 v37, v37\n"
      "v_mfma_f32_16x16x32_bf16 v[24:27], a[104:107], a[72:75], v[24:27]\n"
      "v_add_f32_e32 v16, v37, v16\n"
      "v_cvt_pk_bf16_f32 v46, v36, v37\n"
      "v_mfma_f32_16x16x32_bf16 v[24:27], a[108:111], a[76:79], v[24:27]\n"
      "v_exp_f32_e32 v38, v38\n"
      "v_mfma_f32_16x16x32_bf16 v[28:31], a[112:115], a[64:67], v[64:67]\n"
      "v_add_f32_e32 v16, v38, v16\n"
      "v_mfma_f32_16x16x32_bf16 v[28:31], a[116:119], a[68:71], v[28:31]\n"
      "v_exp_f32_e32 v39, v39\n"
      "v_mfma_f32_16x16x32_bf16 v[28:31], a[120:123], a[72:75], v[28:31]\n"
      "v_add_f32_e32 v16, v39, v16\n"
      "v_cvt_pk_bf16_f32 v47, v38, v39\n"
      "v_mfma_f32_16x16x32_bf16 v[28:31], a[124:127], a[76:79], v[28:31]\n"
      "v_exp_f32_e32 v24, v24\n"
      "v_mfma_f32_16x16x32_bf16 v[160:163], a[160:163], v[44:47], v[160:163]\n"
      "v_add_f32_e32 v17, v24, v17\n"
      "v_mfma_f32_16x16x32_bf16 v[164:167], a[164:167], v[44:47], v[164:167]\n"
      "v_exp_f32_e32 v25, v25\n"
      "v_mfma_f32_16x16x32_bf16 v[168:171], a[168:171], v[44:47], v[168:171]\n"
      "s_add_u32 m0, s40, 50048\n"
      "s_nop 0\n"
      "buffer_load_dwordx4 v7, s[20:23], s38 offen offset:128 nt lds\n"
      "v_add_f32_e32 v17, v25, v17\n"
      "v_cvt_pk_bf16_f32 v40, v24, v25\n"
      "v_mfma_f32_16x16x32_bf16 v[172:175], a[172:175], v[44:47], v[172:175]\n"
      "v_exp_f32_e32 v26, v26\n"
      "v_mfma_f32_16x16x32_bf16 v[176:179], a[176:179], v[44:47], v[176:179]\n"
      "v_add_f32_e32 v17, v26, v17\n"
      "v_mfma_f32_16x16x32_bf16 v[180:183], a[180:183], v[44:47], v[180:183]\n"
      "v_exp_f32_e32 v27, v27\n"
      "v_mfma_f32_16x16x32_bf16 v[184:187], a[184:187], v[44:47], v[184:187]\n"
      "v_add_f32_e32 v17, v27, v17\n"
      "v_cvt_pk_bf16_f32 v41, v26, v27\n"
      "v_mfma_f32_16x16x32_bf16 v[188:191], a[188:191], v[44:47], v[188:191]\n"
      "v_exp_f32_e32 v28, v28\n"
      "v_mfma_f32_16x16x32_bf16 v[32:35], a[96:99], a[80:83], v[68:71]\n"
      "v_add_f32_e32 v17, v28, v17\n"
      "v_mfma_f32_16x16x32_bf16 v[32:35], a[100:103], a[84:87], v[32:35]\n"
      "v_exp_f32_e32 v29, v29\n"
      "v_mfma_f32_16x16x32_bf16 v[32:35], a[104:107], a[88:91], v[32:35]\n"
      "v_add_f32_e32 v17, v29, v17\n"
      "v_cvt_pk_bf16_f32 v42, v28, v29\n"
      "v_mfma_f32_16x16x32_bf16 v[32:35], a[108:111], a[92:95], v[32:35]\n"
      "s_waitcnt vmcnt(12)\n"
      "s_barrier\n"
      "v_exp_f32_e32 v30, v30\n"
      "v_mfma_f32_16x16x32_bf16 v[36:39], a[112:115], a[80:83], v[68:71]\n"
      "ds_read_b128 a[128:131], v1 offset:24576\n"
      "ds_read_b128 a[132:135], v2 offset:24576\n"
      "v_add_f32_e32 v17, v30, v17\n"
      "v_mfma_f32_16x16x32_bf16 v[36:39], a[116:119], a[84:87], v[36:39]\n"
      "v_exp_f32_e32 v31, v31\n"
      "v_mfma_f32_16x16x32_bf16 v[36:39], a[120:123], a[88:91], v[36:39]\n"
      "v_add_f32_e32 v17, v31, v17\n"
      "v_cvt_pk_bf16_f32 v43, v30, v31\n"
      "v_mfma_f32_16x16x32_bf16 v[36:39], a[124:127], a[92:95], v[36:39]\n"
      "v_exp_f32_e32 v32, v32\n"
      "v_mfma_f32_16x16x32_bf16 v[192:195], a[160:163], v[40:43], v[192:195]\n"
      "ds_read_b128 a[136:139], v1 offset:25600\n"
      "ds_read_b128 a[140:143], v2 offset:25600\n"
      "s_add_u32 m0, s40, 114688\n"
      "s_nop 0\n"
      "buffer_load_dwordx4 v8, s[24:27], s38 offen nt lds\n"
      "v_add_f32_e32 v18, v32, v18\n"
      "v_mfma_f32_16x16x32_bf16 v[196:199], a[164:167], v[40:43], v[196:199]\n"
      "v_exp_f32_e32 v33, v33\n"
      "v_mfma_f32_16x16x32_bf16 v[200:203], a[168:171], v[40:43], v[200:203]\n"
      "v_add_f32_e32 v18, v33, v18\n"
      "v_cvt_pk_bf16_f32 v44, v32, v33\n"
      "v_mfma_f32_16x16x32_bf16 v[204:207], a[172:175], v[40:43], v[204:207]\n"
      "ds_read_b128 a[144:147], v1 offset:28672\n"
      "v_exp_f32_e32 v34, v34\n"
      "v_mfma_f32_16x16x32_bf16 v[208:211], a[176:179], v[40:43], v[208:211]\n"
      "ds_read_b128 a[148:151], v2 offset:28672\n"
      "ds_read_b128 a[152:155], v1 offset:29696\n"
      "v_add_f32_e32 v18, v34, v18\n"
      "v_mfma_f32_16x16x32_bf16 v[212:215], a[180:183], v[40:43], v[212:215]\n"
      "ds_read_b128 a[156:159], v2 offset:29696\n"
      "v_exp_f32_e32 v35, v35\n"
      "v_mfma_f32_16x16x32_bf16 v[216:219], a[184:187], v[40:43], v[216:219]\n"
      "v_add_f32_e32 v18, v35, v18\n"
      "v_cvt_pk_bf16_f32 v45, v34, v35\n"
      "v_mfma_f32_16x16x32_bf16 v[220:223], a[188:191], v[40:43], v[220:223]\n"
      "v_exp_f32_e32 v36, v36\n"
      "s_waitcnt lgkmcnt(3)\n"
      "v_mfma_f32_16x16x32_bf16 v[24:27], a[128:131], a[0:3], v[48:51]\n"
      "v_add_f32_e32 v18, v36, v18\n"
      "v_mfma_f32_16x16x32_bf16 v[24:27], a[132:135], a[4:7], v[24:27]\n"
      "v_exp_f32_e32 v37, v37\n"
      "v_mfma_f32_16x16x32_bf16 v[24:27], a[136:139], a[8:11], v[24:27]\n"
      "v_add_f32_e32 v18, v37, v18\n"
      "v_cvt_pk_bf16_f32 v46, v36, v37\n"
      "v_mfma_f32_16x16x32_bf16 v[24:27], a[140:143], a[12:15], v[24:27]\n"
      "s_waitcnt vmcnt(11)\n"
      "s_barrier\n"
      "v_exp_f32_e32 v38, v38\n"
      "v_mfma_f32_16x16x32_bf16 v[28:31], a[144:147], a[0:3], v[48:51]\n"
      "ds_read_b64_tr_b16 a[192:193], v3 offset:24576\n"
      "ds_read_b64_tr_b16 a[194:195], v3 offset:28672\n"
      "v_add_f32_e32 v18, v38, v18\n"
      "s_waitcnt lgkmcnt(2)\n"
      "v_mfma_f32_16x16x32_bf16 v[28:31], a[148:151], a[4:7], v[28:31]\n"
      "ds_read_b64_tr_b16 a[196:197], v4 offset:24576\n"
      "v_exp_f32_e32 v39, v39\n"
      "v_mfma_f32_16x16x32_bf16 v[28:31], a[152:155], a[8:11], v[28:31]\n"
      "v_add_f32_e32 v18, v39, v18\n"
      "v_cvt_pk_bf16_f32 v47, v38, v39\n"
      "v_mfma_f32_16x16x32_bf16 v[28:31], a[156:159], a[12:15], v[28:31]\n"
      "ds_read_b64_tr_b16 a[198:199], v4 offset:28672\n"
      "v_exp_f32_e32 v24, v24\n"
      "v_mfma_f32_16x16x32_bf16 v[224:227], a[160:163], v[44:47], v[224:227]\n"
      "ds_read_b64_tr_b16 a[200:201], v5 offset:24576\n"
      "ds_read_b64_tr_b16 a[202:203], v5 offset:28672\n"
      "s_add_u32 m0, s40, 115584\n"
      "s_nop 0\n"
      "buffer_load_dwordx4 v8, s[24:27], s38 offen offset:128 nt lds\n"
      "v_add_f32_e32 v13, v24, v13\n"
      "v_mfma_f32_16x16x32_bf16 v[228:231], a[164:167], v[44:47], v[228:231]\n"
      "ds_read_b64_tr_b16 a[204:205], v6 offset:24576\n"
      "v_exp_f32_e32 v25, v25\n"
      "v_mfma_f32_16x16x32_bf16 v[232:235], a[168:171], v[44:47], v[232:235]\n"
      "v_add_f32_e32 v13, v25, v13\n"
      "v_cvt_pk_bf16_f32 v40, v24, v25\n"
      "v_mfma_f32_16x16x32_bf16 v[236:239], a[172:175], v[44:47], v[236:239]\n"
      "ds_read_b64_tr_b16 a[206:207], v6 offset:28672\n"
      "v_exp_f32_e32 v26, v26\n"
      "v_mfma_f32_16x16x32_bf16 v[240:243], a[176:179], v[44:47], v[240:243]\n"
      "ds_read_b64_tr_b16 a[208:209], v3 offset:25600\n"
      "ds_read_b64_tr_b16 a[210:211], v3 offset:29696\n"
      "v_add_f32_e32 v13, v26, v13\n"
      "v_mfma_f32_16x16x32_bf16 v[244:247], a[180:183], v[44:47], v[244:247]\n"
      "ds_read_b64_tr_b16 a[212:213], v4 offset:25600\n"
      "v_exp_f32_e32 v27, v27\n"
      "v_mfma_f32_16x16x32_bf16 v[248:251], a[184:187], v[44:47], v[248:251]\n"
      "v_add_f32_e32 v13, v27, v13\n"
      "v_cvt_pk_bf16_f32 v41, v26, v27\n"
      "v_mfma_f32_16x16x32_bf16 v[252:255], a[188:191], v[44:47], v[252:255]\n"
      "ds_read_b64_tr_b16 a[214:215], v4 offset:29696\n"
      "v_exp_f32_e32 v28, v28\n"
      "v_mfma_f32_16x16x32_bf16 v[32:35], a[128:131], a[16:19], v[52:55]\n"
      "ds_read_b64_tr_b16 a[216:217], v5 offset:25600\n"
      "ds_read_b64_tr_b16 a[218:219], v5 offset:29696\n"
      "v_add_f32_e32 v13, v28, v13\n"
      "v_mfma_f32_16x16x32_bf16 v[32:35], a[132:135], a[20:23], v[32:35]\n"
      "ds_read_b64_tr_b16 a[220:221], v6 offset:25600\n"
      "v_exp_f32_e32 v29, v29\n"
      "v_mfma_f32_16x16x32_bf16 v[32:35], a[136:139], a[24:27], v[32:35]\n"
      "v_add_f32_e32 v13, v29, v13\n"
      "v_cvt_pk_bf16_f32 v42, v28, v29\n"
      "v_mfma_f32_16x16x32_bf16 v[32:35], a[140:143], a[28:31], v[32:35]\n"
      "ds_read_b64_tr_b16 a[222:223], v6 offset:29696\n"
      "v_exp_f32_e32 v30, v30\n"
      "v_mfma_f32_16x16x32_bf16 v[36:39], a[144:147], a[16:19], v[52:55]\n"
      "v_add_f32_e32 v13, v30, v13\n"
      "v_mfma_f32_16x16x32_bf16 v[36:39], a[148:151], a[20:23], v[36:39]\n"
      "v_exp_f32_e32 v31, v31\n"
      "v_mfma_f32_16x16x32_bf16 v[36:39], a[152:155], a[24:27], v[36:39]\n"
      "s_add_u32 m0, s40, 57344\n"
      "s_mov_b32 s38, 57344\n"
      "buffer_load_dwordx4 v7, s[20:23], s38 offen nt lds\n"
      "v_add_f32_e32 v13, v31, v13\n"
      "v_cvt_pk_bf16_f32 v43, v30, v31\n"
      "v_mfma_f32_16x16x32_bf16 v[36:39], a[156:159], a[28:31], v[36:39]\n"
      "v_exp_f32_e32 v32, v32\n"
      "s_waitcnt lgkmcnt(0)\n"
      "v_mfma_f32_16x16x32_bf16 a[224:227], a[192:195], v[40:43], a[224:227]\n"
      "v_add_f32_e32 v14, v32, v14\n"
      "v_mfma_f32_16x16x32_bf16 a[228:231], a[196:199], v[40:43], a[228:231]\n"
      "v_exp_f32_e32 v33, v33\n"
      "v_mfma_f32_16x16x32_bf16 a[232:235], a[200:203], v[40:43], a[232:235]\n"
      "v_add_f32_e32 v14, v33, v14\n"
      "v_cvt_pk_bf16_f32 v44, v32, v33\n"
      "v_mfma_f32_16x16x32_bf16 a[236:239], a[204:207], v[40:43], a[236:239]\n"
      "v_exp_f32_e32 v34, v34\n"
      "v_mfma_f32_16x16x32_bf16 a[240:243], a[208:211], v[40:43], a[240:243]\n"
      "v_add_f32_e32 v14, v34, v14\n"
      "v_mfma_f32_16x16x32_bf16 a[244:247], a[212:215], v[40:43], a[244:247]\n"
      "v_exp_f32_e32 v35, v35\n"
      "v_mfma_f32_16x16x32_bf16 a[248:251], a[216:219], v[40:43], a[248:251]\n"
      "v_add_f32_e32 v14, v35, v14\n"
      "v_cvt_pk_bf16_f32 v45, v34, v35\n"
      "v_mfma_f32_16x16x32_bf16 a[252:255], a[220:223], v[40:43], a[252:255]\n"
      "v_exp_f32_e32 v36, v36\n"
      "v_mfma_f32_16x16x32_bf16 v[24:27], a[128:131], a[32:35], v[56:59]\n"
      "v_add_f32_e32 v14, v36, v14\n"
      "v_mfma_f32_16x16x32_bf16 v[24:27], a[132:135], a[36:39], v[24:27]\n"
      "v_exp_f32_e32 v37, v37\n"
      "v_mfma_f32_16x16x32_bf16 v[24:27], a[136:139], a[40:43], v[24:27]\n"
      "v_add_f32_e32 v14, v37, v14\n"
      "v_cvt_pk_bf16_f32 v46, v36, v37\n"
      "v_mfma_f32_16x16x32_bf16 v[24:27], a[140:143], a[44:47], v[24:27]\n"
      "v_exp_f32_e32 v38, v38\n"
      "v_mfma_f32_16x16x32_bf16 v[28:31], a[144:147], a[32:35], v[56:59]\n"
      "v_add_f32_e32 v14, v38, v14\n"
      "v_mfma_f32_16x16x32_bf16 v[28:31], a[148:151], a[36:39], v[28:31]\n"
      "v_exp_f32_e32 v39, v39\n"
      "v_mfma_f32_16x16x32_bf16 v[28:31], a[152:155], a[40:43], v[28:31]\n"
      "s_add_u32 m0, s40, 58240\n"
      "s_nop 0\n"
      "buffer_load_dwordx4 v7, s[20:23], s38 offen offset:128 nt lds\n"
      "v_add_f32_e32 v14, v39, v14\n"
      "v_cvt_pk_bf16_f32 v47, v38, v39\n"
      "v_mfma_f32_16x16x32_bf16 v[28:31], a[156:159], a[44:47], v[28:31]\n"
      "v_exp_f32_e32 v24, v24\n"
      "v_mfma_f32_16x16x32_bf16 v[96:99], a[192:195], v[44:47], v[96:99]\n"
      "v_add_f32_e32 v15, v24, v15\n"
      "v_mfma_f32_16x16x32_bf16 v[100:103], a[196:199], v[44:47], v[100:103]\n"
      "v_exp_f32_e32 v25, v25\n"
      "v_mfma_f32_16x16x32_bf16 v[104:107], a[200:203], v[44:47], v[104:107]\n"
      "v_add_f32_e32 v15, v25, v15\n"
      "v_cvt_pk_bf16_f32 v40, v24, v25\n"
      "v_mfma_f32_16x16x32_bf16 v[108:111], a[204:207], v[44:47], v[108:111]\n"
      "v_exp_f32_e32 v26, v26\n"
      "v_mfma_f32_16x16x32_bf16 v[112:115], a[208:211], v[44:47], v[112:115]\n"
      "v_add_f32_e32 v15, v26, v15\n"
      "v_mfma_f32_16x16x32_bf16 v[116:119], a[212:215], v[44:47], v[116:119]\n"
      "v_exp_f32_e32 v27, v27\n"
      "v_mfma_f32_16x16x32_bf16 v[120:123], a[216:219], v[44:47], v[120:123]\n"
      "v_add_f32_e32 v15, v27, v15\n"
      "v_cvt_pk_bf16_f32 v41, v26, v27\n"
      "v_mfma_f32_16x16x32_bf16 v[124:127], a[220:223], v[44:47], v[124:127]\n"
      "v_exp_f32_e32 v28, v28\n"
      "v_mfma_f32_16x16x32_bf16 v[32:35], a[128:131], a[48:51], v[60:63]\n"
      "v_add_f32_e32 v15, v28, v15\n"
      "v_mfma_f32_16x16x32_bf16 v[32:35], a[132:135], a[52:55], v[32:35]\n"
      "v_exp_f32_e32 v29, v29\n"
      "v_mfma_f32_16x16x32_bf16 v[32:35], a[136:139], a[56:59], v[32:35]\n"
      "v_add_f32_e32 v15, v29, v15\n"
      "v_cvt_pk_bf16_f32 v42, v28, v29\n"
      "v_mfma_f32_16x16x32_bf16 v[32:35], a[140:143], a[60:63], v[32:35]\n"
      "v_exp_f32_e32 v30, v30\n"
      "v_mfma_f32_16x16x32_bf16 v[36:39], a[144:147], a[48:51], v[60:63]\n"
      "s_add_u32 m0, s40, 122880\n"
      "s_nop 0\n"
      "buffer_load_dwordx4 v8, s[24:27], s38 offen nt lds\n"
      "v_add_f32_e32 v15, v30, v15\n"
      "v_mfma_f32_16x16x32_bf16 v[36:39], a[148:151], a[52:55], v[36:39]\n"
      "v_exp_f32_e32 v31, v31\n"
      "v_mfma_f32_16x16x32_bf16 v[36:39], a[152:155], a[56:59], v[36:39]\n"
      "v_add_f32_e32 v15, v31, v15\n"
      "v_cvt_pk_bf16_f32 v43, v30, v31\n"
      "v_mfma_f32_16x16x32_bf16 v[36:39], a[156:159], a[60:63], v[36:39]\n"
      "v_exp_f32_e32 v32, v32\n"
      "v_mfma_f32_16x16x32_bf16 v[128:131], a[192:195], v[40:43], v[128:131]\n"
      "v_add_f32_e32 v16, v32, v16\n"
      "v_mfma_f32_16x16x32_bf16 v[132:135], a[196:199], v[40:43], v[132:135]\n"
      "v_exp_f32_e32 v33, v33\n"
      "v_mfma_f32_16x16x32_bf16 v[136:139], a[200:203], v[40:43], v[136:139]\n"
      "v_add_f32_e32 v16, v33, v16\n"
      "v_cvt_pk_bf16_f32 v44, v32, v33\n"
      "v_mfma_f32_16x16x32_bf16 v[140:143], a[204:207], v[40:43], v[140:143]\n"
      "v_exp_f32_e32 v34, v34\n"
      "v_mfma_f32_16x16x32_bf16 v[144:147], a[208:211], v[40:43], v[144:147]\n"
      "v_add_f32_e32 v16, v34, v16\n"
      "v_mfma_f32_16x16x32_bf16 v[148:151], a[212:215], v[40:43], v[148:151]\n"
      "v_exp_f32_e32 v35, v35\n"
      "v_mfma_f32_16x16x32_bf16 v[152:155], a[216:219], v[40:43], v[152:155]\n"
      "v_add_f32_e32 v16, v35, v16\n"
      "v_cvt_pk_bf16_f32 v45, v34, v35\n"
      "v_mfma_f32_16x16x32_bf16 v[156:159], a[220:223], v[40:43], v[156:159]\n"
      "v_exp_f32_e32 v36, v36\n"
      "v_mfma_f32_16x16x32_bf16 v[24:27], a[128:131], a[64:67], v[64:67]\n"
      "v_add_f32_e32 v16, v36, v16\n"
      "v_mfma_f32_16x16x32_bf16 v[24:27], a[132:135], a[68:71], v[24:27]\n"
      "v_exp_f32_e32 v37, v37\n"
      "v_mfma_f32_16x16x32_bf16 v[24:27], a[136:139], a[72:75], v[24:27]\n"
      "v_add_f32_e32 v16, v37, v16\n"
      "v_cvt_pk_bf16_f32 v46, v36, v37\n"
      "v_mfma_f32_16x16x32_bf16 v[24:27], a[140:143], a[76:79], v[24:27]\n"
      "v_exp_f32_e32 v38, v38\n"
      "v_mfma_f32_16x16x32_bf16 v[28:31], a[144:147], a[64:67], v[64:67]\n"
      "s_add_u32 m0, s40, 123776\n"
      "s_nop 0\n"
      "buffer_load_dwordx4 v8, s[24:27], s38 offen offset:128 nt lds\n"
      "v_add_f32_e32 v16, v38, v16\n"
      "v_mfma_f32_16x16x32_bf16 v[28:31], a[148:151], a[68:71], v[28:31]\n"
      "v_exp_f32_e32 v39, v39\n"
      "v_mfma_f32_16x16x32_bf16 v[28:31], a[152:155], a[72:75], v[28:31]\n"
      "v_add_f32_e32 v16, v39, v16\n"
      "v_cvt_pk_bf16_f32 v47, v38, v39\n"
      "v_mfma_f32_16x16x32_bf16 v[28:31], a[156:159], a[76:79], v[28:31]\n"
      "v_exp_f32_e32 v24, v24\n"
      "v_mfma_f32_16x16x32_bf16 v[160:163], a[192:195], v[44:47], v[160:163]\n"
      "v_add_f32_e32 v17, v24, v17\n"
      "v_mfma_f32_16x16x32_bf16 v[164:167], a[196:199], v[44:47], v[164:167]\n"
      "v_exp_f32_e32 v25, v25\n"
      "v_mfma_f32_16x16x32_bf16 v[168:171], a[200:203], v[44:47], v[168:171]\n"
      "v_add_f32_e32 v17, v25, v17\n"
      "v_cvt_pk_bf16_f32 v40, v24, v25\n"
      "v_mfma_f32_16x16x32_bf16 v[172:175], a[204:207], v[44:47], v[172:175]\n"
      "v_exp_f32_e32 v26, v26\n"
      "v_mfma_f32_16x16x32_bf16 v[176:179], a[208:211], v[44:47], v[176:179]\n"
      "v_add_f32_e32 v17, v26, v17\n"
      "v_mfma_f32_16x16x32_bf16 v[180:183], a[212:215], v[44:47], v[180:183]\n"
      "v_exp_f32_e32 v27, v27\n"
      "v_mfma_f32_16x16x32_bf16 v[184:187], a[216:219], v[44:47], v[184:187]\n"
      "v_add_f32_e32 v17, v27, v17\n"
      "v_cvt_pk_bf16_f32 v41, v26, v27\n"
      "v_mfma_f32_16x16x32_bf16 v[188:191], a[220:223], v[44:47], v[188:191]\n"
      "v_exp_f32_e32 v28, v28\n"
      "v_mfma_f32_16x16x32_bf16 v[32:35], a[128:131], a[80:83], v[68:71]\n"
      "v_add_f32_e32 v17, v28, v17\n"
      "v_mfma_f32_16x16x32_bf16 v[32:35], a[132:135], a[84:87], v[32:35]\n"
      "v_exp_f32_e32 v29, v29\n"
      "v_mfma_f32_16x16x32_bf16 v[32:35], a[136:139], a[88:91], v[32:35]\n"
      "v_add_f32_e32 v17, v29, v17\n"
      "v_cvt_pk_bf16_f32 v42, v28, v29\n"
      "v_mfma_f32_16x16x32_bf16 v[32:35], a[140:143], a[92:95], v[32:35]\n"
      "s_waitcnt vmcnt(12)\n"
      "s_barrier\n"
      "v_exp_f32_e32 v30, v30\n"
      "v_mfma_f32_16x16x32_bf16 v[36:39], a[144:147], a[80:83], v[68:71]\n"
      "ds_read_b128 a[96:99], v1 offset:32768\n"
      "ds_read_b128 a[100:103], v2 offset:32768\n"
      "s_add_u32 m0, s40, 0\n"
      "s_mov_b32 s38, 65536\n"
      "buffer_load_dwordx4 v7, s[20:23], s38 offen nt lds\n"
      "v_add_f32_e32 v17, v30, v17\n"
      "v_mfma_f32_16x16x32_bf16 v[36:39], a[148:151], a[84:87], v[36:39]\n"
      "v_exp_f32_e32 v31, v31\n"
      "v_mfma_f32_16x16x32_bf16 v[36:39], a[152:155], a[88:91], v[36:39]\n"
      "v_add_f32_e32 v17, v31, v17\n"
      "v_cvt_pk_bf16_f32 v43, v30, v31\n"
      "v_mfma_f32_16x16x32_bf16 v[36:39], a[156:159], a[92:95], v[36:39]\n"
      "v_exp_f32_e32 v32, v32\n"
      "v_mfma_f32_16x16x32_bf16 v[192:195], a[192:195], v[40:43], v[192:195]\n"
      "ds_read_b128 a[104:107], v1 offset:33792\n"
      "ds_read_b128 a[108:111], v2 offset:33792\n"
      "v_add_f32_e32 v18, v32, v18\n"
      "v_mfma_f32_16x16x32_bf16 v[196:199], a[196:199], v[40:43], v[196:199]\n"
      "v_exp_f32_e32 v33, v33\n"
      "v_mfma_f32_16x16x32_bf16 v[200:203], a[200:203], v[40:43], v[200:203]\n"
      "v_add_f32_e32 v18, v33, v18\n"
      "v_cvt_pk_bf16_f32 v44, v32, v33\n"
      "v_mfma_f32_16x16x32_bf16 v[204:207], a[204:207], v[40:43], v[204:207]\n"
      "ds_read_b128 a[112:115], v1 offset:36864\n"
      "v_exp_f32_e32 v34, v34\n"
      "v_mfma_f32_16x16x32_bf16 v[208:211], a[208:211], v[40:43], v[208:211]\n"
      "ds_read_b128 a[116:119], v2 offset:36864\n"
      "ds_read_b128 a[120:123], v1 offset:37888\n"
      "v_add_f32_e32 v18, v34, v18\n"
      "v_mfma_f32_16x16x32_bf16 v[212:215], a[212:215], v[40:43], v[212:215]\n"
      "ds_read_b128 a[124:127], v2 offset:37888\n"
      "v_exp_f32_e32 v35, v35\n"
      "v_mfma_f32_16x16x32_bf16 v[216:219], a[216:219], v[40:43], v[216:219]\n"
      "v_add_f32_e32 v18, v35, v18\n"
      "v_cvt_pk_bf16_f32 v45, v34, v35\n"
      "v_mfma_f32_16x16x32_bf16 v[220:223], a[220:223], v[40:43], v[220:223]\n"
      "v_exp_f32_e32 v36, v36\n"
      "s_waitcnt lgkmcnt(3)\n"
      "v_mfma_f32_16x16x32_bf16 v[24:27], a[96:99], a[0:3], v[48:51]\n"
      "v_add_f32_e32 v18, v36, v18\n"
      "v_mfma_f32_16x16x32_bf16 v[24:27], a[100:103], a[4:7], v[24:27]\n"
      "v_exp_f32_e32 v37, v37\n"
      "v_mfma_f32_16x16x32_bf16 v[24:27], a[104:107], a[8:11], v[24:27]\n"
      "s_add_u32 m0, s40, 896\n"
      "s_nop 0\n"
      "buffer_load_dwordx4 v7, s[20:23], s38 offen offset:128 nt lds\n"
      "v_add_f32_e32 v18, v37, v18\n"
      "v_cvt_pk_bf16_f32 v46, v36, v37\n"
      "v_mfma_f32_16x16x32_bf16 v[24:27], a[108:111], a[12:15], v[24:27]\n"
      "v_exp_f32_e32 v38, v38\n"
      "v_mfma_f32_16x16x32_bf16 v[28:31], a[112:115], a[0:3], v[48:51]\n"
      "ds_read_b64_tr_b16 a[160:161], v3 offset:32768\n"
      "ds_read_b64_tr_b16 a[162:163], v3 offset:36864\n"
      "v_add_f32_e32 v18, v38, v18\n"
      "s_waitcnt lgkmcnt(2)\n"
      "v_mfma_f32_16x16x32_bf16 v[28:31], a[116:119], a[4:7], v[28:31]\n"
      "ds_read_b64_tr_b16 a[164:165], v4 offset:32768\n"
      "v_exp_f32_e32 v39, v39\n"
      "v_mfma_f32_16x16x32_bf16 v[28:31], a[120:123], a[8:11], v[28:31]\n"
      "v_add_f32_e32 v18, v39, v18\n"
      "v_cvt_pk_bf16_f32 v47, v38, v39\n"
      "v_mfma_f32_16x16x32_bf16 v[28:31], a[124:127], a[12:15], v[28:31]\n"
      "ds_read_b64_tr_b16 a[166:167], v4 offset:36864\n"
      "v_exp_f32_e32 v24, v24\n"
      "v_mfma_f32_16x16x32_bf16 v[224:227], a[192:195], v[44:47], v[224:227]\n"
      "ds_read_b64_tr_b16 a[168:169], v5 offset:32768\n"
      "ds_read_b64_tr_b16 a[170:171], v5 offset:36864\n"
      "v_add_f32_e32 v13, v24, v13\n"
      "v_mfma_f32_16x16x32_bf16 v[228:231], a[196:199], v[44:47], v[228:231]\n"
      "ds_read_b64_tr_b16 a[172:173], v6 offset:32768\n"
      "v_exp_f32_e32 v25, v25\n"
      "v_mfma_f32_16x16x32_bf16 v[232:235], a[200:203], v[44:47], v[232:235]\n"
      "v_add_f32_e32 v13, v25, v13\n"
      "v_cvt_pk_bf16_f32 v40, v24, v25\n"
      "v_mfma_f32_16x16x32_bf16 v[236:239], a[204:207], v[44:47], v[236:239]\n"
      "ds_read_b64_tr_b16 a[174:175], v6 offset:36864\n"
      "v_exp_f32_e32 v26, v26\n"
      "v_mfma_f32_16x16x32_bf16 v[240:243], a[208:211], v[44:47], v[240:243]\n"
      "ds_read_b64_tr_b16 a[176:177], v3 offset:33792\n"
      "ds_read_b64_tr_b16 a[178:179], v3 offset:37888\n"
      "v_add_f32_e32 v13, v26, v13\n"
      "v_mfma_f32_16x16x32_bf16 v[244:247], a[212:215], v[44:47], v[244:247]\n"
      "ds_read_b64_tr_b16 a[180:181], v4 offset:33792\n"
      "v_exp_f32_e32 v27, v27\n"
      "v_mfma_f32_16x16x32_bf16 v[248:251], a[216:219], v[44:47], v[248:251]\n"
      "v_add_f32_e32 v13, v27, v13\n"
      "v_cvt_pk_bf16_f32 v41, v26, v27\n"
      "v_mfma_f32_16x16x32_bf16 v[252:255], a[220:223], v[44:47], v[252:255]\n"
      "ds_read_b64_tr_b16 a[182:183], v4 offset:37888\n"
      "v_exp_f32_e32 v28, v28\n"
      "v_mfma_f32_16x16x32_bf16 v[32:35], a[96:99], a[16:19], v[52:55]\n"
      "ds_read_b64_tr_b16 a[184:185], v5 offset:33792\n"
      "ds_read_b64_tr_b16 a[186:187], v5 offset:37888\n"
      "s_add_u32 m0, s40, 65536\n"
      "s_nop 0\n"
      "buffer_load_dwordx4 v8, s[24:27], s38 offen nt lds\n"
      "v_add_f32_e32 v13, v28, v13\n"
      "v_mfma_f32_16x16x32_bf16 v[32:35], a[100:103], a[20:23], v[32:35]\n"
      "ds_read_b64_tr_b16 a[188:189], v6 offset:33792\n"
      "v_exp_f32_e32 v29, v29\n"
      "v_mfma_f32_16x16x32_bf16 v[32:35], a[104:107], a[24:27], v[32:35]\n"
      "v_add_f32_e32 v13, v29, v13\n"
      "v_cvt_pk_bf16_f32 v42, v28, v29\n"
      "v_mfma_f32_16x16x32_bf16 v[32:35], a[108:111], a[28:31], v[32:35]\n"
      "ds_read_b64_tr_b16 a[190:191], v6 offset:37888\n"
      "s_waitcnt vmcnt(11)\n"
      "s_barrier\n"
      "v_exp_f32_e32 v30, v30\n"
      "v_mfma_f32_16x16x32_bf16 v[36:39], a[112:115], a[16:19], v[52:55]\n"
      "ds_read_b64_tr_b16 a[192:193], v3 offset:40960\n"
      "v_add_f32_e32 v13, v30, v13\n"
      "v_mfma_f32_16x16x32_bf16 v[36:39], a[116:119], a[20:23], v[36:39]\n"
      "v_exp_f32_e32 v31, v31\n"
      "v_mfma_f32_16x16x32_bf16 v[36:39], a[120:123], a[24:27], v[36:39]\n"
      "v_add_f32_e32 v13, v31, v13\n"
      "v_cvt_pk_bf16_f32 v43, v30, v31\n"
      "v_mfma_f32_16x16x32_bf16 v[36:39], a[124:127], a[28:31], v[36:39]\n"
      "v_exp_f32_e32 v32, v32\n"
      "s_waitcnt lgkmcnt(1)\n"
      "v_mfma_f32_16x16x32_bf16 a[224:227], a[160:163], v[40:43], a[224:227]\n"
      "ds_read_b64_tr_b16 a[194:195], v3 offset:45056\n"
      "v_add_f32_e32 v14, v32, v14\n"
      "v_mfma_f32_16x16x32_bf16 a[228:231], a[164:167], v[40:43], a[228:231]\n"
      "v_exp_f32_e32 v33, v33\n"
      "v_mfma_f32_16x16x32_bf16 a[232:235], a[168:171], v[40:43], a[232:235]\n"
      "v_add_f32_e32 v14, v33, v14\n"
      "v_cvt_pk_bf16_f32 v44, v32, v33\n"
      "v_mfma_f32_16x16x32_bf16 a[236:239], a[172:175], v[40:43], a[236:239]\n"
      "v_exp_f32_e32 v34, v34\n"
      "v_mfma_f32_16x16x32_bf16 a[240:243], a[176:179], v[40:43], a[240:243]\n"
      "ds_read_b64_tr_b16 a[196:197], v4 offset:40960\n"
      "v_add_f32_e32 v14, v34, v14\n"
      "v_mfma_f32_16x16x32_bf16 a[244:247], a[180:183], v[40:43], a[244:247]\n"
      "v_exp_f32_e32 v35, v35\n"
      "v_mfma_f32_16x16x32_bf16 a[248:251], a[184:187], v[40:43], a[248:251]\n"
      "v_add_f32_e32 v14, v35, v14\n"
      "v_cvt_pk_bf16_f32 v45, v34, v35\n"
      "v_mfma_f32_16x16x32_bf16 a[252:255], a[188:191], v[40:43], a[252:255]\n"
      "v_exp_f32_e32 v36, v36\n"
      "v_mfma_f32_16x16x32_bf16 v[24:27], a[96:99], a[32:35], v[56:59]\n"
      "ds_read_b64_tr_b16 a[198:199], v4 offset:45056\n"
      "s_add_u32 m0, s40, 66432\n"
      "s_nop 0\n"
      "buffer_load_dwordx4 v8, s[24:27], s38 offen offset:128 nt lds\n"
      "v_add_f32_e32 v14, v36, v14\n"
      "v_mfma_f32_16x16x32_bf16 v[24:27], a[100:103], a[36:39], v[24:27]\n"
      "v_exp_f32_e32 v37, v37\n"
      "v_mfma_f32_16x16x32_bf16 v[24:27], a[104:107], a[40:43], v[24:27]\n"
      "v_add_f32_e32 v14, v37, v14\n"
      "v_cvt_pk_bf16_f32 v46, v36, v37\n"
      "v_mfma_f32_16x16x32_bf16 v[24:27], a[108:111], a[44:47], v[24:27]\n"
      "v_exp_f32_e32 v38, v38\n"
      "v_mfma_f32_16x16x32_bf16 v[28:31], a[112:115], a[32:35], v[56:59]\n"
      "ds_read_b64_tr_b16 a[200:201], v5 offset:40960\n"
      "v_add_f32_e32 v14, v38, v14\n"
      "v_mfma_f32_16x16x32_bf16 v[28:31], a[116:119], a[36:39], v[28:31]\n"
      "v_exp_f32_e32 v39, v39\n"
      "v_mfma_f32_16x16x32_bf16 v[28:31], a[120:123], a[40:43], v[28:31]\n"
      "v_add_f32_e32 v14, v39, v14\n"
      "v_cvt_pk_bf16_f32 v47, v38, v39\n"
      "v_mfma_f32_16x16x32_bf16 v[28:31], a[124:127], a[44:47], v[28:31]\n"
      "v_exp_f32_e32 v24, v24\n"
      "v_mfma_f32_16x16x32_bf16 v[96:99], a[160:163], v[44:47], v[96:99]\n"
      "ds_read_b64_tr_b16 a[202:203], v5 offset:45056\n"
      "v_add_f32_e32 v15, v24, v15\n"
      "v_mfma_f32_16x16x32_bf16 v[100:103], a[164:167], v[44:47], v[100:103]\n"
      "v_exp_f32_e32 v25, v25\n"
      "v_mfma_f32_16x16x32_bf16 v[104:107], a[168:171], v[44:47], v[104:107]\n"
      "v_add_f32_e32 v15, v25, v15\n"
      "v_cvt_pk_bf16_f32 v40, v24, v25\n"
      "v_mfma_f32_16x16x32_bf16 v[108:111], a[172:175], v[44:47], v[108:111]\n"
      "v_exp_f32_e32 v26, v26\n"
      "v_mfma_f32_16x16x32_bf16 v[112:115], a[176:179], v[44:47], v[112:115]\n"
      "ds_read_b64_tr_b16 a[204:205], v6 offset:40960\n"
      "v_add_f32_e32 v15, v26, v15\n"
      "v_mfma_f32_16x16x32_bf16 v[116:119], a[180:183], v[44:47], v[116:119]\n"
      "v_exp_f32_e32 v27, v27\n"
      "v_mfma_f32_16x16x32_bf16 v[120:123], a[184:187], v[44:47], v[120:123]\n"
      "s_add_u32 m0, s40, 8192\n"
      "s_mov_b32 s38, 73728\n"
      "buffer_load_dwordx4 v7, s[20:23], s38 offen nt lds\n"
      "v_add_f32_e32 v15, v27, v15\n"
      "v_cvt_pk_bf16_f32 v41, v26, v27\n"
      "v_mfma_f32_16x16x32_bf16 v[124:127], a[188:191], v[44:47], v[124:127]\n"
      "v_exp_f32_e32 v28, v28\n"
      "v_mfma_f32_16x16x32_bf16 v[32:35], a[96:99], a[48:51], v[60:63]\n"
      "ds_read_b64_tr_b16 a[206:207], v6 offset:45056\n"
      "v_add_f32_e32 v15, v28, v15\n"
      "v_mfma_f32_16x16x32_bf16 v[32:35], a[100:103], a[52:55], v[32:35]\n"
      "v_exp_f32_e32 v29, v29\n"
      "v_mfma_f32_16x16x32_bf16 v[32:35], a[104:107], a[56:59], v[32:35]\n"
      "v_add_f32_e32 v15, v29, v15\n"
      "v_cvt_pk_bf16_f32 v42, v28, v29\n"
      "v_mfma_f32_16x16x32_bf16 v[32:35], a[108:111], a[60:63], v[32:35]\n"
      "v_exp_f32_e32 v30, v30\n"
      "v_mfma_f32_16x16x32_bf16 v[36:39], a[112:115], a[48:51], v[60:63]\n"
      "ds_read_b64_tr_b16 a[208:209], v3 offset:41984\n"
      "v_add_f32_e32 v15, v30, v15\n"
      "v_mfma_f32_16x16x32_bf16 v[36:39], a[116:119], a[52:55], v[36:39]\n"
      "v_exp_f32_e32 v31, v31\n"
      "v_mfma_f32_16x16x32_bf16 v[36:39], a[120:123], a[56:59], v[36:39]\n"
      "v_add_f32_e32 v15, v31, v15\n"
      "v_cvt_pk_bf16_f32 v43, v30, v31\n"
      "v_mfma_f32_16x16x32_bf16 v[36:39], a[124:127], a[60:63], v[36:39]\n"
      "v_exp_f32_e32 v32, v32\n"
      "v_mfma_f32_16x16x32_bf16 v[128:131], a[160:163], v[40:43], v[128:131]\n"
      "ds_read_b64_tr_b16 a[210:211], v3 offset:46080\n"
      "v_add_f32_e32 v16, v32, v16\n"
      "v_mfma_f32_16x16x32_bf16 v[132:135], a[164:167], v[40:43], v[132:135]\n"
      "v_exp_f32_e32 v33, v33\n"
      "v_mfma_f32_16x16x32_bf16 v[136:139], a[168:171], v[40:43], v[136:139]\n"
      "v_add_f32_e32 v16, v33, v16\n"
      "v_cvt_pk_bf16_f32 v44, v32, v33\n"
      "v_mfma_f32_16x16x32_bf16 v[140:143], a[172:175], v[40:43], v[140:143]\n"
      "v_exp_f32_e32 v34, v34\n"
      "v_mfma_f32_16x16x32_bf16 v[144:147], a[176:179], v[40:43], v[144:147]\n"
      "ds_read_b64_tr_b16 a[212:213], v4 offset:41984\n"
      "v_add_f32_e32 v16, v34, v16\n"
      "v_mfma_f32_16x16x32_bf16 v[148:151], a[180:183], v[40:43], v[148:151]\n"
      "v_exp_f32_e32 v35, v35\n"
      "v_mfma_f32_16x16x32_bf16 v[152:155], a[184:187], v[40:43], v[152:155]\n"
      "s_add_u32 m0, s40, 9088\n"
      "s_nop 0\n"
      "buffer_load_dwordx4 v7, s[20:23], s38 offen offset:128 nt lds\n"
      "v_add_f32_e32 v16, v35, v16\n"
      "v_cvt_pk_bf16_f32 v45, v34, v35\n"
      "v_mfma_f32_16x16x32_bf16 v[156:159], a[188:191], v[40:43], v[156:159]\n"
      "v_exp_f32_e32 v36, v36\n"
      "v_mfma_f32_16x16x32_bf16 v[24:27], a[96:99], a[64:67], v[64:67]\n"
      "ds_read_b128 a[128:131], v1 offset:40960\n"
      "v_add_f32_e32 v16, v36, v16\n"
      "v_mfma_f32_16x16x32_bf16 v[24:27], a[100:103], a[68:71], v[24:27]\n"
      "v_exp_f32_e32 v37, v37\n"
      "v_mfma_f32_16x16x32_bf16 v[24:27], a[104:107], a[72:75], v[24:27]\n"
      "v_add_f32_e32 v16, v37, v16\n"
      "v_cvt_pk_bf16_f32 v46, v36, v37\n"
      "v_mfma_f32_16x16x32_bf16 v[24:27], a[108:111], a[76:79], v[24:27]\n"
      "v_exp_f32_e32 v38, v38\n"
      "v_mfma_f32_16x16x32_bf16 v[28:31], a[112:115], a[64:67], v[64:67]\n"
      "ds_read_b128 a[132:135], v2 offset:40960\n"
      "v_add_f32_e32 v16, v38, v16\n"
      "v_mfma_f32_16x16x32_bf16 v[28:31], a[116:119], a[68:71], v[28:31]\n"
      "v_exp_f32_e32 v39, v39\n"
      "v_mfma_f32_16x16x32_bf16 v[28:31], a[120:123], a[72:75], v[28:31]\n"
      "v_add_f32_e32 v16, v39, v16\n"
      "v_cvt_pk_bf16_f32 v47, v38, v39\n"
      "v_mfma_f32_16x16x32_bf16 v[28:31], a[124:127], a[76:79], v[28:31]\n"
      "v_exp_f32_e32 v24, v24\n"
      "v_mfma_f32_16x16x32_bf16 v[160:163], a[160:163], v[44:47], v[160:163]\n"
      "ds_read_b128 a[136:139], v1 offset:41984\n"
      "v_add_f32_e32 v17, v24, v17\n"
      "v_mfma_f32_16x16x32_bf16 v[164:167], a[164:167], v[44:47], v[164:167]\n"
      "v_exp_f32_e32 v25, v25\n"
      "v_mfma_f32_16x16x32_bf16 v[168:171], a[168:171], v[44:47], v[168:171]\n"
      "v_add_f32_e32 v17, v25, v17\n"
      "v_cvt_pk_bf16_f32 v40, v24, v25\n"
      "v_mfma_f32_16x16x32_bf16 v[172:175], a[172:175], v[44:47], v[172:175]\n"
      "v_exp_f32_e32 v26, v26\n"
      "v_mfma_f32_16x16x32_bf16 v[176:179], a[176:179], v[44:47], v[176:179]\n"
      "ds_read_b128 a[140:143], v2 offset:41984\n"
      "v_add_f32_e32 v17, v26, v17\n"
      "v_mfma_f32_16x16x32_bf16 v[180:183], a[180:183], v[44:47], v[180:183]\n"
      "v_exp_f32_e32 v27, v27\n"
      "v_mfma_f32_16x16x32_bf16 v[184:187], a[184:187], v[44:47], v[184:187]\n"
      "s_add_u32 m0, s40, 73728\n"
      "s_nop 0\n"
      "buffer_load_dwordx4 v8, s[24:27], s38 offen nt lds\n"
      "v_add_f32_e32 v17, v27, v17\n"
      "v_cvt_pk_bf16_f32 v41, v26, v27\n"
      "v_mfma_f32_16x16x32_bf16 v[188:191], a[188:191], v[44:47], v[188:191]\n"
      "v_exp_f32_e32 v28, v28\n"
      "v_mfma_f32_16x16x32_bf16 v[32:35], a[96:99], a[80:83], v[68:71]\n"
      "ds_read_b128 a[144:147], v1 offset:45056\n"
      "v_add_f32_e32 v17, v28, v17\n"
      "v_mfma_f32_16x16x32_bf16 v[32:35], a[100:103], a[84:87], v[32:35]\n"
      "v_exp_f32_e32 v29, v29\n"
      "v_mfma_f32_16x16x32_bf16 v[32:35], a[104:107], a[88:91], v[32:35]\n"
      "v_add_f32_e32 v17, v29, v17\n"
      "v_cvt_pk_bf16_f32 v42, v28, v29\n"
      "v_mfma_f32_16x16x32_bf16 v[32:35], a[108:111], a[92:95], v[32:35]\n"
      "v_exp_f32_e32 v30, v30\n"
      "v_mfma_f32_16x16x32_bf16 v[36:39], a[112:115], a[80:83], v[68:71]\n"
      "ds_read_b128 a[148:151], v2 offset:45056\n"
      "v_add_f32_e32 v17, v30, v17\n"
      "v_mfma_f32_16x16x32_bf16 v[36:39], a[116:119], a[84:87], v[36:39]\n"
      "v_exp_f32_e32 v31, v31\n"
      "v_mfma_f32_16x16x32_bf16 v[36:39], a[120:123], a[88:91], v[36:39]\n"
      "v_add_f32_e32 v17, v31, v17\n"
      "v_cvt_pk_bf16_f32 v43, v30, v31\n"
      "v_mfma_f32_16x16x32_bf16 v[36:39], a[124:127], a[92:95], v[36:39]\n"
      "v_exp_f32_e32 v32, v32\n"
      "v_mfma_f32_16x16x32_bf16 v[192:195], a[160:163], v[40:43], v[192:195]\n"
      "ds_read_b128 a[152:155], v1 offset:46080\n"
      "v_add_f32_e32 v18, v32, v18\n"
      "v_mfma_f32_16x16x32_bf16 v[196:199], a[164:167], v[40:43], v[196:199]\n"
      "v_exp_f32_e32 v33, v33\n"
      "v_mfma_f32_16x16x32_bf16 v[200:203], a[168:171], v[40:43], v[200:203]\n"
      "v_add_f32_e32 v18, v33, v18\n"
      "v_cvt_pk_bf16_f32 v44, v32, v33\n"
      "v_mfma_f32_16x16x32_bf16 v[204:207], a[172:175], v[40:43], v[204:207]\n"
      "v_exp_f32_e32 v34, v34\n"
      "v_mfma_f32_16x16x32_bf16 v[208:211], a[176:179], v[40:43], v[208:211]\n"
      "ds_read_b128 a[156:159], v2 offset:46080\n"
      "s_add_u32 m0, s40, 74624\n"
      "s_nop 0\n"
      "buffer_load_dwordx4 v8, s[24:27], s38 offen offset:128 nt lds\n"
      "v_add_f32_e32 v18, v34, v18\n"
      "v_mfma_f32_16x16x32_bf16 v[212:215], a[180:183], v[40:43], v[212:215]\n"
      "v_exp_f32_e32 v35, v35\n"
      "v_mfma_f32_16x16x32_bf16 v[216:219], a[184:187], v[40:43], v[216:219]\n"
      "v_add_f32_e32 v18, v35, v18\n"
      "v_cvt_pk_bf16_f32 v45, v34, v35\n"
      "v_mfma_f32_16x16x32_bf16 v[220:223], a[188:191], v[40:43], v[220:223]\n"
      "v_exp_f32_e32 v36, v36\n"
      "s_waitcnt lgkmcnt(1)\n"
      "v_mfma_f32_16x16x32_bf16 v[24:27], a[128:131], a[0:3], v[48:51]\n"
      "ds_read_b64_tr_b16 a[214:215], v4 offset:46080\n"
      "v_add_f32_e32 v18, v36, v18\n"
      "v_mfma_f32_16x16x32_bf16 v[24:27], a[132:135], a[4:7], v[24:27]\n"
      "v_exp_f32_e32 v37, v37\n"
      "v_mfma_f32_16x16x32_bf16 v[24:27], a[136:139], a[8:11], v[24:27]\n"
      "v_add_f32_e32 v18, v37, v18\n"
      "v_cvt_pk_bf16_f32 v46, v36, v37\n"
      "v_mfma_f32_16x16x32_bf16 v[24:27], a[140:143], a[12:15], v[24:27]\n"
      "v_exp_f32_e32 v38, v38\n"
      "v_mfma_f32_16x16x32_bf16 v[28:31], a[144:147], a[0:3], v[48:51]\n"
      "ds_read_b64_tr_b16 a[216:217], v5 offset:41984\n"
      "v_add_f32_e32 v18, v38, v18\n"
      "v_mfma_f32_16x16x32_bf16 v[28:31], a[148:151], a[4:7], v[28:31]\n"
      "v_exp_f32_e32 v39, v39\n"
      "v_mfma_f32_16x16x32_bf16 v[28:31], a[152:155], a[8:11], v[28:31]\n"
      "v_add_f32_e32 v18, v39, v18\n"
      "v_cvt_pk_bf16_f32 v47, v38, v39\n"
      "s_waitcnt lgkmcnt(1)\n"
      "v_mfma_f32_16x16x32_bf16 v[28:31], a[156:159], a[12:15], v[28:31]\n"
      "v_exp_f32_e32 v24, v24\n"
      "v_mfma_f32_16x16x32_bf16 v[224:227], a[160:163], v[44:47], v[224:227]\n"
      "ds_read_b64_tr_b16 a[218:219], v5 offset:46080\n"
      "v_add_f32_e32 v13, v24, v13\n"
      "v_mfma_f32_16x16x32_bf16 v[228:231], a[164:167], v[44:47], v[228:231]\n"
      "v_exp_f32_e32 v25, v25\n"
      "v_mfma_f32_16x16x32_bf16 v[232:235], a[168:171], v[44:47], v[232:235]\n"
      "v_add_f32_e32 v13, v25, v13\n"
      "v_cvt_pk_bf16_f32 v40, v24, v25\n"
      "v_mfma_f32_16x16x32_bf16 v[236:239], a[172:175], v[44:47], v[236:239]\n"
      "v_exp_f32_e32 v26, v26\n"
      "v_mfma_f32_16x16x32_bf16 v[240:243], a[176:179], v[44:47], v[240:243]\n"
      "ds_read_b64_tr_b16 a[220:221], v6 offset:41984\n"
      "s_add_u32 m0, s40, 16384\n"
      "s_mov_b32 s38, 81920\n"
      "buffer_load_dwordx4 v7, s[20:23], s38 offen nt lds\n"
      "v_add_f32_e32 v13, v26, v13\n"
      "v_mfma_f32_16x16x32_bf16 v[244:247], a[180:183], v[44:47], v[244:247]\n"
      "v_exp_f32_e32 v27, v27\n"
      "v_mfma_f32_16x16x32_bf16 v[248:251], a[184:187], v[44:47], v[248:251]\n"
      "v_add_f32_e32 v13, v27, v13\n"
      "v_cvt_pk_bf16_f32 v41, v26, v27\n"
      "v_mfma_f32_16x16x32_bf16 v[252:255], a[188:191], v[44:47], v[252:255]\n"
      "v_exp_f32_e32 v28, v28\n"
      "v_mfma_f32_16x16x32_bf16 v[32:35], a[128:131], a[16:19], v[52:55]\n"
      "ds_read_b64_tr_b16 a[222:223], v6 offset:46080\n"
      "v_add_f32_e32 v13, v28, v13\n"
      "v_mfma_f32_16x16x32_bf16 v[32:35], a[132:135], a[20:23], v[32:35]\n"
      "v_exp_f32_e32 v29, v29\n"
      "v_mfma_f32_16x16x32_bf16 v[32:35], a[136:139], a[24:27], v[32:35]\n"
      "v_add_f32_e32 v13, v29, v13\n"
      "v_cvt_pk_bf16_f32 v42, v28, v29\n"
      "v_mfma_f32_16x16x32_bf16 v[32:35], a[140:143], a[28:31], v[32:35]\n"
      "s_waitcnt vmcnt(13)\n"
      "s_barrier\n"
      "v_exp_f32_e32 v30, v30\n"
      "v_mfma_f32_16x16x32_bf16 v[36:39], a[144:147], a[16:19], v[52:55]\n"
      "ds_read_b64_tr_b16 a[160:161], v3 offset:49152\n"
      "v_add_f32_e32 v13, v30, v13\n"
      "v_mfma_f32_16x16x32_bf16 v[36:39], a[148:151], a[20:23], v[36:39]\n"
      "v_exp_f32_e32 v31, v31\n"
      "v_mfma_f32_16x16x32_bf16 v[36:39], a[152:155], a[24:27], v[36:39]\n"
      "v_add_f32_e32 v13, v31, v13\n"
      "v_cvt_pk_bf16_f32 v43, v30, v31\n"
      "v_mfma_f32_16x16x32_bf16 v[36:39], a[156:159], a[28:31], v[36:39]\n"
      "v_exp_f32_e32 v32, v32\n"
      "v_mfma_f32_16x16x32_bf16 a[224:227], a[192:195], v[40:43], a[224:227]\n"
      "ds_read_b64_tr_b16 a[162:163], v3 offset:53248\n"
      "v_add_f32_e32 v14, v32, v14\n"
      "v_mfma_f32_16x16x32_bf16 a[228:231], a[196:199], v[40:43], a[228:231]\n"
      "v_exp_f32_e32 v33, v33\n"
      "v_mfma_f32_16x16x32_bf16 a[232:235], a[200:203], v[40:43], a[232:235]\n"
      "v_add_f32_e32 v14, v33, v14\n"
      "v_cvt_pk_bf16_f32 v44, v32, v33\n"
      "v_mfma_f32_16x16x32_bf16 a[236:239], a[204:207], v[40:43], a[236:239]\n"
      "v_exp_f32_e32 v34, v34\n"
      "v_mfma_f32_16x16x32_bf16 a[240:243], a[208:211], v[40:43], a[240:243]\n"
      "ds_read_b64_tr_b16 a[164:165], v4 offset:49152\n"
      "s_add_u32 m0, s40, 17280\n"
      "s_nop 0\n"
      "buffer_load_dwordx4 v7, s[20:23], s38 offen offset:128 nt lds\n"
      "v_add_f32_e32 v14, v34, v14\n"
      "v_mfma_f32_16x16x32_bf16 a[244:247], a[212:215], v[40:43], a[244:247]\n"
      "v_exp_f32_e32 v35, v35\n"
      "s_waitcnt lgkmcnt(1)\n"
      "v_mfma_f32_16x16x32_bf16 a[248:251], a[216:219], v[40:43], a[248:251]\n"
      "v_add_f32_e32 v14, v35, v14\n"
      "v_cvt_pk_bf16_f32 v45, v34, v35\n"
      "v_mfma_f32_16x16x32_bf16 a[252:255], a[220:223], v[40:43], a[252:255]\n"
      "v_exp_f32_e32 v36, v36\n"
      "v_mfma_f32_16x16x32_bf16 v[24:27], a[128:131], a[32:35], v[56:59]\n"
      "ds_read_b64_tr_b16 a[166:167], v4 offset:53248\n"
      "v_add_f32_e32 v14, v36, v14\n"
      "v_mfma_f32_16x16x32_bf16 v[24:27], a[132:135], a[36:39], v[24:27]\n"
      "v_exp_f32_e32 v37, v37\n"
      "v_mfma_f32_16x16x32_bf16 v[24:27], a[136:139], a[40:43], v[24:27]\n"
      "v_add_f32_e32 v14, v37, v14\n"
      "v_cvt_pk_bf16_f32 v46, v36, v37\n"
      "v_mfma_f32_16x16x32_bf16 v[24:27], a[140:143], a[44:47], v[24:27]\n"
      "v_exp_f32_e32 v38, v38\n"
      "v_mfma_f32_16x16x32_bf16 v[28:31], a[144:147], a[32:35], v[56:59]\n"
      "ds_read_b64_tr_b16 a[168:169], v5 offset:49152\n"
      "v_add_f32_e32 v14, v38, v14\n"
      "v_mfma_f32_16x16x32_bf16 v[28:31], a[148:151], a[36:39], v[28:31]\n"
      "v_exp_f32_e32 v39, v39\n"
      "v_mfma_f32_16x16x32_bf16 v[28:31], a[152:155], a[40:43], v[28:31]\n"
      "v_add_f32_e32 v14, v39, v14\n"
      "v_cvt_pk_bf16_f32 v47, v38, v39\n"
      "v_mfma_f32_16x16x32_bf16 v[28:31], a[156:159], a[44:47], v[28:31]\n"
      "v_exp_f32_e32 v24, v24\n"
      "v_mfma_f32_16x16x32_bf16 v[96:99], a[192:195], v[44:47], v[96:99]\n"
      "ds_read_b64_tr_b16 a[170:171], v5 offset:53248\n"
      "v_add_f32_e32 v15, v24, v15\n"
      "v_mfma_f32_16x16x32_bf16 v[100:103], a[196:199], v[44:47], v[100:103]\n"
      "v_exp_f32_e32 v25, v25\n"
      "v_mfma_f32_16x16x32_bf16 v[104:107], a[200:203], v[44:47], v[104:107]\n"
      "s_add_u32 m0, s40, 81920\n"
      "s_nop 0\n"
      "buffer_load_dwordx4 v8, s[24:27], s38 offen nt lds\n"
      "v_add_f32_e32 v15, v25, v15\n"
      "v_cvt_pk_bf16_f32 v40, v24, v25\n"
      "v_mfma_f32_16x16x32_bf16 v[108:111], a[204:207], v[44:47], v[108:111]\n"
      "v_exp_f32_e32 v26, v26\n"
      "v_mfma_f32_16x16x32_bf16 v[112:115], a[208:211], v[44:47], v[112:115]\n"
      "ds_read_b64_tr_b16 a[172:173], v6 offset:49152\n"
      "v_add_f32_e32 v15, v26, v15\n"
      "v_mfma_f32_16x16x32_bf16 v[116:119], a[212:215], v[44:47], v[116:119]\n"
      "v_exp_f32_e32 v27, v27\n"
      "v_mfma_f32_16x16x32_bf16 v[120:123], a[216:219], v[44:47], v[120:123]\n"
      "v_add_f32_e32 v15, v27, v15\n"
      "v_cvt_pk_bf16_f32 v41, v26, v27\n"
      "v_mfma_f32_16x16x32_bf16 v[124:127], a[220:223], v[44:47], v[124:127]\n"
      "v_exp_f32_e32 v28, v28\n"
      "v_mfma_f32_16x16x32_bf16 v[32:35], a[128:131], a[48:51], v[60:63]\n"
      "ds_read_b64_tr_b16 a[174:175], v6 offset:53248\n"
      "v_add_f32_e32 v15, v28, v15\n"
      "v_mfma_f32_16x16x32_bf16 v[32:35], a[132:135], a[52:55], v[32:35]\n"
      "v_exp_f32_e32 v29, v29\n"
      "v_mfma_f32_16x16x32_bf16 v[32:35], a[136:139], a[56:59], v[32:35]\n"
      "v_add_f32_e32 v15, v29, v15\n"
      "v_cvt_pk_bf16_f32 v42, v28, v29\n"
      "v_mfma_f32_16x16x32_bf16 v[32:35], a[140:143], a[60:63], v[32:35]\n"
      "v_exp_f32_e32 v30, v30\n"
      "v_mfma_f32_16x16x32_bf16 v[36:39], a[144:147], a[48:51], v[60:63]\n"
      "ds_read_b64_tr_b16 a[176:177], v3 offset:50176\n"
      "v_add_f32_e32 v15, v30, v15\n"
      "v_mfma_f32_16x16x32_bf16 v[36:39], a[148:151], a[52:55], v[36:39]\n"
      "v_exp_f32_e32 v31, v31\n"
      "v_mfma_f32_16x16x32_bf16 v[36:39], a[152:155], a[56:59], v[36:39]\n"
      "v_add_f32_e32 v15, v31, v15\n"
      "v_cvt_pk_bf16_f32 v43, v30, v31\n"
      "v_mfma_f32_16x16x32_bf16 v[36:39], a[156:159], a[60:63], v[36:39]\n"
      "v_exp_f32_e32 v32, v32\n"
      "v_mfma_f32_16x16x32_bf16 v[128:131], a[192:195], v[40:43], v[128:131]\n"
      "ds_read_b64_tr_b16 a[178:179], v3 offset:54272\n"
      "v_add_f32_e32 v16, v32, v16\n"
      "v_mfma_f32_16x16x32_bf16 v[132:135], a[196:199], v[40:43], v[132:135]\n"
      "v_exp_f32_e32 v33, v33\n"
      "v_mfma_f32_16x16x32_bf16 v[136:139], a[200:203], v[40:43], v[136:139]\n"
      "s_add_u32 m0, s40, 82816\n"
      "s_nop 0\n"
      "buffer_load_dwordx4 v8, s[24:27], s38 offen offset:128 nt lds\n"
      "v_add_f32_e32 v16, v33, v16\n"
      "v_cvt_pk_bf16_f32 v44, v32, v33\n"
      "v_mfma_f32_16x16x32_bf16 v[140:143], a[204:207], v[40:43], v[140:143]\n"
      "v_exp_f32_e32 v34, v34\n"
      "v_mfma_f32_16x16x32_bf16 v[144:147], a[208:211], v[40:43], v[144:147]\n"
      "ds_read_b64_tr_b16 a[180:181], v4 offset:50176\n"
      "v_add_f32_e32 v16, v34, v16\n"
      "v_mfma_f32_16x16x32_bf16 v[148:151], a[212:215], v[40:43], v[148:151]\n"
      "v_exp_f32_e32 v35, v35\n"
      "v_mfma_f32_16x16x32_bf16 v[152:155], a[216:219], v[40:43], v[152:155]\n"
      "v_add_f32_e32 v16, v35, v16\n"
      "v_cvt_pk_bf16_f32 v45, v34, v35\n"
      "v_mfma_f32_16x16x32_bf16 v[156:159], a[220:223], v[40:43], v[156:159]\n"
      "v_exp_f32_e32 v36, v36\n"
      "v_mfma_f32_16x16x32_bf16 v[24:27], a[128:131], a[64:67], v[64:67]\n"
      "ds_read_b128 a[96:99], v1 offset:49152\n"
      "v_add_f32_e32 v16, v36, v16\n"
      "v_mfma_f32_16x16x32_bf16 v[24:27], a[132:135], a[68:71], v[24:27]\n"
      "v_exp_f32_e32 v37, v37\n"
      "v_mfma_f32_16x16x32_bf16 v[24:27], a[136:139], a[72:75], v[24:27]\n"
      "v_add_f32_e32 v16, v37, v16\n"
      "v_cvt_pk_bf16_f32 v46, v36, v37\n"
      "v_mfma_f32_16x16x32_bf16 v[24:27], a[140:143], a[76:79], v[24:27]\n"
      "v_exp_f32_e32 v38, v38\n"
      "v_mfma_f32_16x16x32_bf16 v[28:31], a[144:147], a[64:67], v[64:67]\n"
      "ds_read_b128 a[100:103], v2 offset:49152\n"
      "v_add_f32_e32 v16, v38, v16\n"
      "v_mfma_f32_16x16x32_bf16 v[28:31], a[148:151], a[68:71], v[28:31]\n"
      "v_exp_f32_e32 v39, v39\n"
      "v_mfma_f32_16x16x32_bf16 v[28:31], a[152:155], a[72:75], v[28:31]\n"
      "v_add_f32_e32 v16, v39, v16\n"
      "v_cvt_pk_bf16_f32 v47, v38, v39\n"
      "v_mfma_f32_16x16x32_bf16 v[28:31], a[156:159], a[76:79], v[28:31]\n"
      "v_exp_f32_e32 v24, v24\n"
      "v_mfma_f32_16x16x32_bf16 v[160:163], a[192:195], v[44:47], v[160:163]\n"
      "ds_read_b128 a[104:107], v1 offset:50176\n"
      "v_add_f32_e32 v17, v24, v17\n"
      "v_mfma_f32_16x16x32_bf16 v[164:167], a[196:199], v[44:47], v[164:167]\n"
      "v_exp_f32_e32 v25, v25\n"
      "v_mfma_f32_16x16x32_bf16 v[168:171], a[200:203], v[44:47], v[168:171]\n"
      "s_add_u32 m0, s40, 24576\n"
      "s_mov_b32 s38, 90112\n"
      "buffer_load_dwordx4 v7, s[20:23], s38 offen nt lds\n"
      "v_add_f32_e32 v17, v25, v17\n"
      "v_cvt_pk_bf16_f32 v40, v24, v25\n"
      "v_mfma_f32_16x16x32_bf16 v[172:175], a[204:207], v[44:47], v[172:175]\n"
      "v_exp_f32_e32 v26, v26\n"
      "v_mfma_f32_16x16x32_bf16 v[176:179], a[208:211], v[44:47], v[176:179]\n"
      "ds_read_b128 a[108:111], v2 offset:50176\n"
      "v_add_f32_e32 v17, v26, v17\n"
      "v_mfma_f32_16x16x32_bf16 v[180:183], a[212:215], v[44:47], v[180:183]\n"
      "v_exp_f32_e32 v27, v27\n"
      "v_mfma_f32_16x16x32_bf16 v[184:187], a[216:219], v[44:47], v[184:187]\n"
      "v_add_f32_e32 v17, v27, v17\n"
      "v_cvt_pk_bf16_f32 v41, v26, v27\n"
      "v_mfma_f32_16x16x32_bf16 v[188:191], a[220:223], v[44:47], v[188:191]\n"
      "v_exp_f32_e32 v28, v28\n"
      "v_mfma_f32_16x16x32_bf16 v[32:35], a[128:131], a[80:83], v[68:71]\n"
      "ds_read_b128 a[112:115], v1 offset:53248\n"
      "v_add_f32_e32 v17, v28, v17\n"
      "v_mfma_f32_16x16x32_bf16 v[32:35], a[132:135], a[84:87], v[32:35]\n"
      "v_exp_f32_e32 v29, v29\n"
      "v_mfma_f32_16x16x32_bf16 v[32:35], a[136:139], a[88:91], v[32:35]\n"
      "v_add_f32_e32 v17, v29, v17\n"
      "v_cvt_pk_bf16_f32 v42, v28, v29\n"
      "v_mfma_f32_16x16x32_bf16 v[32:35], a[140:143], a[92:95], v[32:35]\n"
      "v_exp_f32_e32 v30, v30\n"
      "v_mfma_f32_16x16x32_bf16 v[36:39], a[144:147], a[80:83], v[68:71]\n"
      "ds_read_b128 a[116:119], v2 offset:53248\n"
      "v_add_f32_e32 v17, v30, v17\n"
      "v_mfma_f32_16x16x32_bf16 v[36:39], a[148:151], a[84:87], v[36:39]\n"
      "v_exp_f32_e32 v31, v31\n"
      "v_mfma_f32_16x16x32_bf16 v[36:39], a[152:155], a[88:91], v[36:39]\n"
      "v_add_f32_e32 v17, v31, v17\n"
      "v_cvt_pk_bf16_f32 v43, v30, v31\n"
      "v_mfma_f32_16x16x32_bf16 v[36:39], a[156:159], a[92:95], v[36:39]\n"
      "v_exp_f32_e32 v32, v32\n"
      "v_mfma_f32_16x16x32_bf16 v[192:195], a[192:195], v[40:43], v[192:195]\n"
      "ds_read_b128 a[120:123], v1 offset:54272\n"
      "s_add_u32 m0, s40, 25472\n"
      "s_nop 0\n"
      "buffer_load_dwordx4 v7, s[20:23], s38 offen offset:128 nt lds\n"
      "v_add_f32_e32 v18, v32, v18\n"
      "v_mfma_f32_16x16x32_bf16 v[196:199], a[196:199], v[40:43], v[196:199]\n"
      "v_exp_f32_e32 v33, v33\n"
      "v_mfma_f32_16x16x32_bf16 v[200:203], a[200:203], v[40:43], v[200:203]\n"
      "v_add_f32_e32 v18, v33, v18\n"
      "v_cvt_pk_bf16_f32 v44, v32, v33\n"
      "v_mfma_f32_16x16x32_bf16 v[204:207], a[204:207], v[40:43], v[204:207]\n"
      "v_exp_f32_e32 v34, v34\n"
      "v_mfma_f32_16x16x32_bf16 v[208:211], a[208:211], v[40:43], v[208:211]\n"
      "ds_read_b128 a[124:127], v2 offset:54272\n"
      "v_add_f32_e32 v18, v34, v18\n"
      "v_mfma_f32_16x16x32_bf16 v[212:215], a[212:215], v[40:43], v[212:215]\n"
      "v_exp_f32_e32 v35, v35\n"
      "v_mfma_f32_16x16x32_bf16 v[216:219], a[216:219], v[40:43], v[216:219]\n"
      "v_add_f32_e32 v18, v35, v18\n"
      "v_cvt_pk_bf16_f32 v45, v34, v35\n"
      "v_mfma_f32_16x16x32_bf16 v[220:223], a[220:223], v[40:43], v[220:223]\n"
      "v_exp_f32_e32 v36, v36\n"
      "s_waitcnt lgkmcnt(1)\n"
      "v_mfma_f32_16x16x32_bf16 v[24:27], a[96:99], a[0:3], v[48:51]\n"
      "ds_read_b64_tr_b16 a[182:183], v4 offset:54272\n"
      "v_add_f32_e32 v18, v36, v18\n"
      "v_mfma_f32_16x16x32_bf16 v[24:27], a[100:103], a[4:7], v[24:27]\n"
      "v_exp_f32_e32 v37, v37\n"
      "v_mfma_f32_16x16x32_bf16 v[24:27], a[104:107], a[8:11], v[24:27]\n"
      "v_add_f32_e32 v18, v37, v18\n"
      "v_cvt_pk_bf16_f32 v46, v36, v37\n"
      "v_mfma_f32_16x16x32_bf16 v[24:27], a[108:111], a[12:15], v[24:27]\n"
      "v_exp_f32_e32 v38, v38\n"
      "v_mfma_f32_16x16x32_bf16 v[28:31], a[112:115], a[0:3], v[48:51]\n"
      "ds_read_b64_tr_b16 a[184:185], v5 offset:50176\n"
      "v_add_f32_e32 v18, v38, v18\n"
      "v_mfma_f32_16x16x32_bf16 v[28:31], a[116:119], a[4:7], v[28:31]\n"
      "v_exp_f32_e32 v39, v39\n"
      "v_mfma_f32_16x16x32_bf16 v[28:31], a[120:123], a[8:11], v[28:31]\n"
      "v_add_f32_e32 v18, v39, v18\n"
      "v_cvt_pk_bf16_f32 v47, v38, v39\n"
      "s_waitcnt lgkmcnt(1)\n"
      "v_mfma_f32_16x16x32_bf16 v[28:31], a[124:127], a[12:15], v[28:31]\n"
      "v_exp_f32_e32 v24, v24\n"
      "v_mfma_f32_16x16x32_bf16 v[224:227], a[192:195], v[44:47], v[224:227]\n"
      "ds_read_b64_tr_b16 a[186:187], v5 offset:54272\n"
      "s_add_u32 m0, s40, 90112\n"
      "s_nop 0\n"
      "buffer_load_dwordx4 v8, s[24:27], s38 offen nt lds\n"
      "v_add_f32_e32 v13, v24, v13\n"
      "v_mfma_f32_16x16x32_bf16 v[228:231], a[196:199], v[44:47], v[228:231]\n"
      "v_exp_f32_e32 v25, v25\n"
      "v_mfma_f32_16x16x32_bf16 v[232:235], a[200:203], v[44:47], v[232:235]\n"
      "v_add_f32_e32 v13, v25, v13\n"
      "v_cvt_pk_bf16_f32 v40, v24, v25\n"
      "v_mfma_f32_16x16x32_bf16 v[236:239], a[204:207], v[44:47], v[236:239]\n"
      "v_exp_f32_e32 v26, v26\n"
      "v_mfma_f32_16x16x32_bf16 v[240:243], a[208:211], v[44:47], v[240:243]\n"
      "ds_read_b64_tr_b16 a[188:189], v6 offset:50176\n"
      "v_add_f32_e32 v13, v26, v13\n"
      "v_mfma_f32_16x16x32_bf16 v[244:247], a[212:215], v[44:47], v[244:247]\n"
      "v_exp_f32_e32 v27, v27\n"
      "v_mfma_f32_16x16x32_bf16 v[248:251], a[216:219], v[44:47], v[248:251]\n"
      "v_add_f32_e32 v13, v27, v13\n"
      "v_cvt_pk_bf16_f32 v41, v26, v27\n"
      "v_mfma_f32_16x16x32_bf16 v[252:255], a[220:223], v[44:47], v[252:255]\n"
      "v_exp_f32_e32 v28, v28\n"
      "v_mfma_f32_16x16x32_bf16 v[32:35], a[96:99], a[16:19], v[52:55]\n"
      "ds_read_b64_tr_b16 a[190:191], v6 offset:54272\n"
      "v_add_f32_e32 v13, v28, v13\n"
      "v_mfma_f32_16x16x32_bf16 v[32:35], a[100:103], a[20:23], v[32:35]\n"
      "v_exp_f32_e32 v29, v29\n"
      "v_mfma_f32_16x16x32_bf16 v[32:35], a[104:107], a[24:27], v[32:35]\n"
      "v_add_f32_e32 v13, v29, v13\n"
      "v_cvt_pk_bf16_f32 v42, v28, v29\n"
      "v_mfma_f32_16x16x32_bf16 v[32:35], a[108:111], a[28:31], v[32:35]\n"
      "s_waitcnt vmcnt(11)\n"
      "s_barrier\n"
      "v_exp_f32_e32 v30, v30\n"
      "v_mfma_f32_16x16x32_bf16 v[36:39], a[112:115], a[16:19], v[52:55]\n"
      "ds_read_b64_tr_b16 a[192:193], v3 offset:57344\n"
      "v_add_f32_e32 v13, v30, v13\n"
      "v_mfma_f32_16x16x32_bf16 v[36:39], a[116:119], a[20:23], v[36:39]\n"
      "v_exp_f32_e32 v31, v31\n"
      "v_mfma_f32_16x16x32_bf16 v[36:39], a[120:123], a[24:27], v[36:39]\n"
      "v_add_f32_e32 v13, v31, v13\n"
      "v_cvt_pk_bf16_f32 v43, v30, v31\n"
      "v_mfma_f32_16x16x32_bf16 v[36:39], a[124:127], a[28:31], v[36:39]\n"
      "v_exp_f32_e32 v32, v32\n"
      "v_mfma_f32_16x16x32_bf16 a[224:227], a[160:163], v[40:43], a[224:227]\n"
      "ds_read_b64_tr_b16 a[194:195], v3 offset:61440\n"
      "s_add_u32 m0, s40, 91008\n"
      "s_nop 0\n"
      "buffer_load_dwordx4 v8, s[24:27], s38 offen offset:128 nt lds\n"
      "v_add_f32_e32 v14, v32, v14\n"
      "v_mfma_f32_16x16x32_bf16 a[228:231], a[164:167], v[40:43], a[228:231]\n"
      "v_exp_f32_e32 v33, v33\n"
      "v_mfma_f32_16x16x32_bf16 a[232:235], a[168:171], v[40:43], a[232:235]\n"
      "v_add_f32_e32 v14, v33, v14\n"
      "v_cvt_pk_bf16_f32 v44, v32, v33\n"
      "v_mfma_f32_16x16x32_bf16 a[236:239], a[172:175], v[40:43], a[236:239]\n"
      "v_exp_f32_e32 v34, v34\n"
      "v_mfma_f32_16x16x32_bf16 a[240:243], a[176:179], v[40:43], a[240:243]\n"
      "ds_read_b64_tr_b16 a[196:197], v4 offset:57344\n"
      "v_add_f32_e32 v14, v34, v14\n"
      "v_mfma_f32_16x16x32_bf16 a[244:247], a[180:183], v[40:43], a[244:247]\n"
      "v_exp_f32_e32 v35, v35\n"
      "s_waitcnt lgkmcnt(1)\n"
      "v_mfma_f32_16x16x32_bf16 a[248:251], a[184:187], v[40:43], a[248:251]\n"
      "v_add_f32_e32 v14, v35, v14\n"
      "v_cvt_pk_bf16_f32 v45, v34, v35\n"
      "v_mfma_f32_16x16x32_bf16 a[252:255], a[188:191], v[40:43], a[252:255]\n"
      "v_exp_f32_e32 v36, v36\n"
      "v_mfma_f32_16x16x32_bf16 v[24:27], a[96:99], a[32:35], v[56:59]\n"
      "ds_read_b64_tr_b16 a[198:199], v4 offset:61440\n"
      "v_add_f32_e32 v14, v36, v14\n"
      "v_mfma_f32_16x16x32_bf16 v[24:27], a[100:103], a[36:39], v[24:27]\n"
      "v_exp_f32_e32 v37, v37\n"
      "v_mfma_f32_16x16x32_bf16 v[24:27], a[104:107], a[40:43], v[24:27]\n"
      "v_add_f32_e32 v14, v37, v14\n"
      "v_cvt_pk_bf16_f32 v46, v36, v37\n"
      "v_mfma_f32_16x16x32_bf16 v[24:27], a[108:111], a[44:47], v[24:27]\n"
      "v_exp_f32_e32 v38, v38\n"
      "v_mfma_f32_16x16x32_bf16 v[28:31], a[112:115], a[32:35], v[56:59]\n"
      "ds_read_b64_tr_b16 a[200:201], v5 offset:57344\n"
      "v_add_f32_e32 v14, v38, v14\n"
      "v_mfma_f32_16x16x32_bf16 v[28:31], a[116:119], a[36:39], v[28:31]\n"
      "v_exp_f32_e32 v39, v39\n"
      "v_mfma_f32_16x16x32_bf16 v[28:31], a[120:123], a[40:43], v[28:31]\n"
      "v_add_f32_e32 v14, v39, v14\n"
      "v_cvt_pk_bf16_f32 v47, v38, v39\n"
      "v_mfma_f32_16x16x32_bf16 v[28:31], a[124:127], a[44:47], v[28:31]\n"
      "v_exp_f32_e32 v24, v24\n"
      "v_mfma_f32_16x16x32_bf16 v[96:99], a[160:163], v[44:47], v[96:99]\n"
      "ds_read_b64_tr_b16 a[202:203], v5 offset:61440\n"
      "v_add_f32_e32 v15, v24, v15\n"
      "v_mfma_f32_16x16x32_bf16 v[100:103], a[164:167], v[44:47], v[100:103]\n"
      "v_exp_f32_e32 v25, v25\n"
      "v_mfma_f32_16x16x32_bf16 v[104:107], a[168:171], v[44:47], v[104:107]\n"
      "v_add_f32_e32 v15, v25, v15\n"
      "v_cvt_pk_bf16_f32 v40, v24, v25\n"
      "v_mfma_f32_16x16x32_bf16 v[108:111], a[172:175], v[44:47], v[108:111]\n"
      "v_exp_f32_e32 v26, v26\n"
      "v_mfma_f32_16x16x32_bf16 v[112:115], a[176:179], v[44:47], v[112:115]\n"
      "ds_read_b64_tr_b16 a[204:205], v6 offset:57344\n"
      "v_add_f32_e32 v15, v26, v15\n"
      "v_mfma_f32_16x16x32_bf16 v[116:119], a[180:183], v[44:47], v[116:119]\n"
      "v_exp_f32_e32 v27, v27\n"
      "v_mfma_f32_16x16x32_bf16 v[120:123], a[184:187], v[44:47], v[120:123]\n"
      "v_add_f32_e32 v15, v27, v15\n"
      "v_cvt_pk_bf16_f32 v41, v26, v27\n"
      "v_mfma_f32_16x16x32_bf16 v[124:127], a[188:191], v[44:47], v[124:127]\n"
      "v_exp_f32_e32 v28, v28\n"
      "v_mfma_f32_16x16x32_bf16 v[32:35], a[96:99], a[48:51], v[60:63]\n"
      "ds_read_b64_tr_b16 a[206:207], v6 offset:61440\n"
      "v_add_f32_e32 v15, v28, v15\n"
      "v_mfma_f32_16x16x32_bf16 v[32:35], a[100:103], a[52:55], v[32:35]\n"
      "v_exp_f32_e32 v29, v29\n"
      "v_mfma_f32_16x16x32_bf16 v[32:35], a[104:107], a[56:59], v[32:35]\n"
      "v_add_f32_e32 v15, v29, v15\n"
      "v_cvt_pk_bf16_f32 v42, v28, v29\n"
      "v_mfma_f32_16x16x32_bf16 v[32:35], a[108:111], a[60:63], v[32:35]\n"
      "v_exp_f32_e32 v30, v30\n"
      "v_mfma_f32_16x16x32_bf16 v[36:39], a[112:115], a[48:51], v[60:63]\n"
      "ds_read_b64_tr_b16 a[208:209], v3 offset:58368\n"
      "v_add_f32_e32 v15, v30, v15\n"
      "v_mfma_f32_16x16x32_bf16 v[36:39], a[116:119], a[52:55], v[36:39]\n"
      "v_exp_f32_e32 v31, v31\n"
      "v_mfma_f32_16x16x32_bf16 v[36:39], a[120:123], a[56:59], v[36:39]\n"
      "v_add_f32_e32 v15, v31, v15\n"
      "v_cvt_pk_bf16_f32 v43, v30, v31\n"
      "v_mfma_f32_16x16x32_bf16 v[36:39], a[124:127], a[60:63], v[36:39]\n"
      "v_exp_f32_e32 v32, v32\n"
      "v_mfma_f32_16x16x32_bf16 v[128:131], a[160:163], v[40:43], v[128:131]\n"
      "ds_read_b64_tr_b16 a[210:211], v3 offset:62464\n"
      "v_add_f32_e32 v16, v32, v16\n"
      "v_mfma_f32_16x16x32_bf16 v[132:135], a[164:167], v[40:43], v[132:135]\n"
      "v_exp_f32_e32 v33, v33\n"
      "v_mfma_f32_16x16x32_bf16 v[136:139], a[168:171], v[40:43], v[136:139]\n"
      "v_add_f32_e32 v16, v33, v16\n"
      "v_cvt_pk_bf16_f32 v44, v32, v33\n"
      "v_mfma_f32_16x16x32_bf16 v[140:143], a[172:175], v[40:43], v[140:143]\n"
      "v_exp_f32_e32 v34, v34\n"
      "v_mfma_f32_16x16x32_bf16 v[144:147], a[176:179], v[40:43], v[144:147]\n"
      "ds_read_b64_tr_b16 a[212:213], v4 offset:58368\n"
      "v_add_f32_e32 v16, v34, v16\n"
      "v_mfma_f32_16x16x32_bf16 v[148:151], a[180:183], v[40:43], v[148:151]\n"
      "v_exp_f32_e32 v35, v35\n"
      "v_mfma_f32_16x16x32_bf16 v[152:155], a[184:187], v[40:43], v[152:155]\n"
      "v_add_f32_e32 v16, v35, v16\n"
      "v_cvt_pk_bf16_f32 v45, v34, v35\n"
      "v_mfma_f32_16x16x32_bf16 v[156:159], a[188:191], v[40:43], v[156:159]\n"
      "v_exp_f32_e32 v36, v36\n"
      "v_mfma_f32_16x16x32_bf16 v[24:27], a[96:99], a[64:67], v[64:67]\n"
      "ds_read_b128 a[128:131], v1 offset:57344\n"
      "v_add_f32_e32 v16, v36, v16\n"
      "v_mfma_f32_16x16x32_bf16 v[24:27], a[100:103], a[68:71], v[24:27]\n"
      "v_exp_f32_e32 v37, v37\n"
      "v_mfma_f32_16x16x32_bf16 v[24:27], a[104:107], a[72:75], v[24:27]\n"
      "v_add_f32_e32 v16, v37, v16\n"
      "v_cvt_pk_bf16_f32 v46, v36, v37\n"
      "v_mfma_f32_16x16x32_bf16 v[24:27], a[108:111], a[76:79], v[24:27]\n"
      "v_exp_f32_e32 v38, v38\n"
      "v_mfma_f32_16x16x32_bf16 v[28:31], a[112:115], a[64:67], v[64:67]\n"
      "ds_read_b128 a[132:135], v2 offset:57344\n"
      "v_add_f32_e32 v16, v38, v16\n"
      "v_mfma_f32_16x16x32_bf16 v[28:31], a[116:119], a[68:71], v[28:31]\n"
      "v_exp_f32_e32 v39, v39\n"
      "v_mfma_f32_16x16x32_bf16 v[28:31], a[120:123], a[72:75], v[28:31]\n"
      "v_add_f32_e32 v16, v39, v16\n"
      "v_cvt_pk_bf16_f32 v47, v38, v39\n"
      "v_mfma_f32_16x16x32_bf16 v[28:31], a[124:127], a[76:79], v[28:31]\n"
      "v_exp_f32_e32 v24, v24\n"
      "v_mfma_f32_16x16x32_bf16 v[160:163], a[160:163], v[44:47], v[160:163]\n"
      "ds_read_b128 a[136:139], v1 offset:58368\n"
      "v_add_f32_e32 v17, v24, v17\n"
      "v_mfma_f32_16x16x32_bf16 v[164:167], a[164:167], v[44:47], v[164:167]\n"
      "v_exp_f32_e32 v25, v25\n"
      "v_mfma_f32_16x16x32_bf16 v[168:171], a[168:171], v[44:47], v[168:171]\n"
      "v_add_f32_e32 v17, v25, v17\n"
      "v_cvt_pk_bf16_f32 v40, v24, v25\n"
      "v_mfma_f32_16x16x32_bf16 v[172:175], a[172:175], v[44:47], v[172:175]\n"
      "v_exp_f32_e32 v26, v26\n"
      "v_mfma_f32_16x16x32_bf16 v[176:179], a[176:179], v[44:47], v[176:179]\n"
      "ds_read_b128 a[140:143], v2 offset:58368\n"
      "v_add_f32_e32 v17, v26, v17\n"
      "v_mfma_f32_16x16x32_bf16 v[180:183], a[180:183], v[44:47], v[180:183]\n"
      "v_exp_f32_e32 v27, v27\n"
      "v_mfma_f32_16x16x32_bf16 v[184:187], a[184:187], v[44:47], v[184:187]\n"
      "v_add_f32_e32 v17, v27, v17\n"
      "v_cvt_pk_bf16_f32 v41, v26, v27\n"
      "v_mfma_f32_16x16x32_bf16 v[188:191], a[188:191], v[44:47], v[188:191]\n"
      "v_exp_f32_e32 v28, v28\n"
      "v_mfma_f32_16x16x32_bf16 v[32:35], a[96:99], a[80:83], v[68:71]\n"
      "ds_read_b128 a[144:147], v1 offset:61440\n"
      "v_add_f32_e32 v17, v28, v17\n"
      "v_mfma_f32_16x16x32_bf16 v[32:35], a[100:103], a[84:87], v[32:35]\n"
      "v_exp_f32_e32 v29, v29\n"
      "v_mfma_f32_16x16x32_bf16 v[32:35], a[104:107], a[88:91], v[32:35]\n"
      "v_add_f32_e32 v17, v29, v17\n"
      "v_cvt_pk_bf16_f32 v42, v28, v29\n"
      "v_mfma_f32_16x16x32_bf16 v[32:35], a[108:111], a[92:95], v[32:35]\n"
      "v_exp_f32_e32 v30, v30\n"
      "v_mfma_f32_16x16x32_bf16 v[36:39], a[112:115], a[80:83], v[68:71]\n"
      "ds_read_b128 a[148:151], v2 offset:61440\n"
      "v_add_f32_e32 v17, v30, v17\n"
      "v_mfma_f32_16x16x32_bf16 v[36:39], a[116:119], a[84:87], v[36:39]\n"
      "v_exp_f32_e32 v31, v31\n"
      "v_mfma_f32_16x16x32_bf16 v[36:39], a[120:123], a[88:91], v[36:39]\n"
      "v_add_f32_e32 v17, v31, v17\n"
      "v_cvt_pk_bf16_f32 v43, v30, v31\n"
      "v_mfma_f32_16x16x32_bf16 v[36:39], a[124:127], a[92:95], v[36:39]\n"
      "v_exp_f32_e32 v32, v32\n"
      "v_mfma_f32_16x16x32_bf16 v[192:195], a[160:163], v[40:43], v[192:195]\n"
      "ds_read_b128 a[152:155], v1 offset:62464\n"
      "v_add_f32_e32 v18, v32, v18\n"
      "v_mfma_f32_16x16x32_bf16 v[196:199], a[164:167], v[40:43], v[196:199]\n"
      "v_exp_f32_e32 v33, v33\n"
      "v_mfma_f32_16x16x32_bf16 v[200:203], a[168:171], v[40:43], v[200:203]\n"
      "v_add_f32_e32 v18, v33, v18\n"
      "v_cvt_pk_bf16_f32 v44, v32, v33\n"
      "v_mfma_f32_16x16x32_bf16 v[204:207], a[172:175], v[40:43], v[204:207]\n"
      "v_exp_f32_e32 v34, v34\n"
      "v_mfma_f32_16x16x32_bf16 v[208:211], a[176:179], v[40:43], v[208:211]\n"
      "ds_read_b128 a[156:159], v2 offset:62464\n"
      "v_add_f32_e32 v18, v34, v18\n"
      "v_mfma_f32_16x16x32_bf16 v[212:215], a[180:183], v[40:43], v[212:215]\n"
      "v_exp_f32_e32 v35, v35\n"
      "v_mfma_f32_16x16x32_bf16 v[216:219], a[184:187], v[40:43], v[216:219]\n"
      "v_add_f32_e32 v18, v35, v18\n"
      "v_cvt_pk_bf16_f32 v45, v34, v35\n"
      "v_mfma_f32_16x16x32_bf16 v[220:223], a[188:191], v[40:43], v[220:223]\n"
      "v_exp_f32_e32 v36, v36\n"
      "s_waitcnt lgkmcnt(1)\n"
      "v_mfma_f32_16x16x32_bf16 v[24:27], a[128:131], a[0:3], v[48:51]\n"
      "ds_read_b64_tr_b16 a[214:215], v4 offset:62464\n"
      "v_add_f32_e32 v18, v36, v18\n"
      "v_mfma_f32_16x16x32_bf16 v[24:27], a[132:135], a[4:7], v[24:27]\n"
      "v_exp_f32_e32 v37, v37\n"
      "v_mfma_f32_16x16x32_bf16 v[24:27], a[136:139], a[8:11], v[24:27]\n"
      "v_add_f32_e32 v18, v37, v18\n"
      "v_cvt_pk_bf16_f32 v46, v36, v37\n"
      "v_mfma_f32_16x16x32_bf16 v[24:27], a[140:143], a[12:15], v[24:27]\n"
      "v_exp_f32_e32 v38, v38\n"
      "v_mfma_f32_16x16x32_bf16 v[28:31], a[144:147], a[0:3], v[48:51]\n"
      "ds_read_b64_tr_b16 a[216:217], v5 offset:58368\n"
      "v_add_f32_e32 v18, v38, v18\n"
      "v_mfma_f32_16x16x32_bf16 v[28:31], a[148:151], a[4:7], v[28:31]\n"
      "v_exp_f32_e32 v39, v39\n"
      "v_mfma_f32_16x16x32_bf16 v[28:31], a[152:155], a[8:11], v[28:31]\n"
      "v_add_f32_e32 v18, v39, v18\n"
      "v_cvt_pk_bf16_f32 v47, v38, v39\n"
      "s_waitcnt lgkmcnt(1)\n"
      "v_mfma_f32_16x16x32_bf16 v[28:31], a[156:159], a[12:15], v[28:31]\n"
      "v_exp_f32_e32 v24, v24\n"
      "v_mfma_f32_16x16x32_bf16 v[224:227], a[160:163], v[44:47], v[224:227]\n"
      "ds_read_b64_tr_b16 a[218:219], v5 offset:62464\n"
      "v_add_f32_e32 v13, v24, v13\n"
      "v_mfma_f32_16x16x32_bf16 v[228:231], a[164:167], v[44:47], v[228:231]\n"
      "v_exp_f32_e32 v25, v25\n"
      "v_mfma_f32_16x16x32_bf16 v[232:235], a[168:171], v[44:47], v[232:235]\n"
      "v_add_f32_e32 v13, v25, v13\n"
      "v_cvt_pk_bf16_f32 v40, v24, v25\n"
      "v_mfma_f32_16x16x32_bf16 v[236:239], a[172:175], v[44:47], v[236:239]\n"
      "v_exp_f32_e32 v26, v26\n"
      "v_mfma_f32_16x16x32_bf16 v[240:243], a[176:179], v[44:47], v[240:243]\n"
      "ds_read_b64_tr_b16 a[220:221], v6 offset:58368\n"
      "v_add_f32_e32 v13, v26, v13\n"
      "v_mfma_f32_16x16x32_bf16 v[244:247], a[180:183], v[44:47], v[244:247]\n"
      "v_exp_f32_e32 v27, v27\n"
      "v_mfma_f32_16x16x32_bf16 v[248:251], a[184:187], v[44:47], v[248:251]\n"
      "v_add_f32_e32 v13, v27, v13\n"
      "v_cvt_pk_bf16_f32 v41, v26, v27\n"
      "v_mfma_f32_16x16x32_bf16 v[252:255], a[188:191], v[44:47], v[252:255]\n"
      "v_exp_f32_e32 v28, v28\n"
      "v_mfma_f32_16x16x32_bf16 v[32:35], a[128:131], a[16:19], v[52:55]\n"
      "ds_read_b64_tr_b16 a[222:223], v6 offset:62464\n"
      "v_add_f32_e32 v13, v28, v13\n"
      "v_mfma_f32_16x16x32_bf16 v[32:35], a[132:135], a[20:23], v[32:35]\n"
      "v_exp_f32_e32 v29, v29\n"
      "v_mfma_f32_16x16x32_bf16 v[32:35], a[136:139], a[24:27], v[32:35]\n"
      "v_add_f32_e32 v13, v29, v13\n"
      "v_cvt_pk_bf16_f32 v42, v28, v29\n"
      "v_mfma_f32_16x16x32_bf16 v[32:35], a[140:143], a[28:31], v[32:35]\n"
      "v_exp_f32_e32 v30, v30\n"
      "v_mfma_f32_16x16x32_bf16 v[36:39], a[144:147], a[16:19], v[52:55]\n"
      "ds_read_b64_tr_b16 a[160:161], v3 offset:0\n"
      "v_add_f32_e32 v13, v30, v13\n"
      "v_mfma_f32_16x16x32_bf16 v[36:39], a[148:151], a[20:23], v[36:39]\n"
      "v_exp_f32_e32 v31, v31\n"
      "v_mfma_f32_16x16x32_bf16 v[36:39], a[152:155], a[24:27], v[36:39]\n"
      "v_add_f32_e32 v13, v31, v13\n"
      "v_cvt_pk_bf16_f32 v43, v30, v31\n"
      "v_mfma_f32_16x16x32_bf16 v[36:39], a[156:159], a[28:31], v[36:39]\n"
      "v_exp_f32_e32 v32, v32\n"
      "v_mfma_f32_16x16x32_bf16 a[224:227], a[192:195], v[40:43], a[224:227]\n"
      "ds_read_b64_tr_b16 a[162:163], v3 offset:4096\n"
      "v_add_f32_e32 v14, v32, v14\n"
      "v_mfma_f32_16x16x32_bf16 a[228:231], a[196:199], v[40:43], a[228:231]\n"
      "v_exp_f32_e32 v33, v33\n"
      "v_mfma_f32_16x16x32_bf16 a[232:235], a[200:203], v[40:43], a[232:235]\n"
      "v_add_f32_e32 v14, v33, v14\n"
      "v_cvt_pk_bf16_f32 v44, v32, v33\n"
      "v_mfma_f32_16x16x32_bf16 a[236:239], a[204:207], v[40:43], a[236:239]\n"
      "v_exp_f32_e32 v34, v34\n"
      "v_mfma_f32_16x16x32_bf16 a[240:243], a[208:211], v[40:43], a[240:243]\n"
      "ds_read_b64_tr_b16 a[164:165], v4 offset:0\n"
      "v_add_f32_e32 v14, v34, v14\n"
      "v_mfma_f32_16x16x32_bf16 a[244:247], a[212:215], v[40:43], a[244:247]\n"
      "v_exp_f32_e32 v35, v35\n"
      "s_waitcnt lgkmcnt(1)\n"
      "v_mfma_f32_16x16x32_bf16 a[248:251], a[216:219], v[40:43], a[248:251]\n"
      "v_add_f32_e32 v14, v35, v14\n"
      "v_cvt_pk_bf16_f32 v45, v34, v35\n"
      "v_mfma_f32_16x16x32_bf16 a[252:255], a[220:223], v[40:43], a[252:255]\n"
      "v_exp_f32_e32 v36, v36\n"
      "v_mfma_f32_16x16x32_bf16 v[24:27], a[128:131], a[32:35], v[56:59]\n"
      "ds_read_b64_tr_b16 a[166:167], v4 offset:4096\n"
      "v_add_f32_e32 v14, v36, v14\n"
      "v_mfma_f32_16x16x32_bf16 v[24:27], a[132:135], a[36:39], v[24:27]\n"
      "v_exp_f32_e32 v37, v37\n"
      "v_mfma_f32_16x16x32_bf16 v[24:27], a[136:139], a[40:43], v[24:27]\n"
      "v_add_f32_e32 v14, v37, v14\n"
      "v_cvt_pk_bf16_f32 v46, v36, v37\n"
      "v_mfma_f32_16x16x32_bf16 v[24:27], a[140:143], a[44:47], v[24:27]\n"
      "v_exp_f32_e32 v38, v38\n"
      "v_mfma_f32_16x16x32_bf16 v[28:31], a[144:147], a[32:35], v[56:59]\n"
      "ds_read_b64_tr_b16 a[168:169], v5 offset:0\n"
      "v_add_f32_e32 v14, v38, v14\n"
      "v_mfma_f32_16x16x32_bf16 v[28:31], a[148:151], a[36:39], v[28:31]\n"
      "v_exp_f32_e32 v39, v39\n"
      "v_mfma_f32_16x16x32_bf16 v[28:31], a[152:155], a[40:43], v[28:31]\n"
      "v_add_f32_e32 v14, v39, v14\n"
      "v_cvt_pk_bf16_f32 v47, v38, v39\n"
      "v_mfma_f32_16x16x32_bf16 v[28:31], a[156:159], a[44:47], v[28:31]\n"
      "v_exp_f32_e32 v24, v24\n"
      "v_mfma_f32_16x16x32_bf16 v[96:99], a[192:195], v[44:47], v[96:99]\n"
      "ds_read_b64_tr_b16 a[170:171], v5 offset:4096\n"
      "v_add_f32_e32 v15, v24, v15\n"
      "v_mfma_f32_16x16x32_bf16 v[100:103], a[196:199], v[44:47], v[100:103]\n"
      "v_exp_f32_e32 v25, v25\n"
      "v_mfma_f32_16x16x32_bf16 v[104:107], a[200:203], v[44:47], v[104:107]\n"
      "v_add_f32_e32 v15, v25, v15\n"
      "v_cvt_pk_bf16_f32 v40, v24, v25\n"
      "v_mfma_f32_16x16x32_bf16 v[108:111], a[204:207], v[44:47], v[108:111]\n"
      "v_exp_f32_e32 v26, v26\n"
      "v_mfma_f32_16x16x32_bf16 v[112:115], a[208:211], v[44:47], v[112:115]\n"
      "ds_read_b64_tr_b16 a[172:173], v6 offset:0\n"
      "v_add_f32_e32 v15, v26, v15\n"
      "v_mfma_f32_16x16x32_bf16 v[116:119], a[212:215], v[44:47], v[116:119]\n"
      "v_exp_f32_e32 v27, v27\n"
      "v_mfma_f32_16x16x32_bf16 v[120:123], a[216:219], v[44:47], v[120:123]\n"
      "v_add_f32_e32 v15, v27, v15\n"
      "v_cvt_pk_bf16_f32 v41, v26, v27\n"
      "v_mfma_f32_16x16x32_bf16 v[124:127], a[220:223], v[44:47], v[124:127]\n"
      "v_exp_f32_e32 v28, v28\n"
      "v_mfma_f32_16x16x32_bf16 v[32:35], a[128:131], a[48:51], v[60:63]\n"
      "ds_read_b64_tr_b16 a[174:175], v6 offset:4096\n"
      "v_add_f32_e32 v15, v28, v15\n"
      "v_mfma_f32_16x16x32_bf16 v[32:35], a[132:135], a[52:55], v[32:35]\n"
      "v_exp_f32_e32 v29, v29\n"
      "v_mfma_f32_16x16x32_bf16 v[32:35], a[136:139], a[56:59], v[32:35]\n"
      "v_add_f32_e32 v15, v29, v15\n"
      "v_cvt_pk_bf16_f32 v42, v28, v29\n"
      "v_mfma_f32_16x16x32_bf16 v[32:35], a[140:143], a[60:63], v[32:35]\n"
      "v_exp_f32_e32 v30, v30\n"
      "v_mfma_f32_16x16x32_bf16 v[36:39], a[144:147], a[48:51], v[60:63]\n"
      "ds_read_b64_tr_b16 a[176:177], v3 offset:1024\n"
      "v_add_f32_e32 v15, v30, v15\n"
      "v_mfma_f32_16x16x32_bf16 v[36:39], a[148:151], a[52:55], v[36:39]\n"
      "v_exp_f32_e32 v31, v31\n"
      "v_mfma_f32_16x16x32_bf16 v[36:39], a[152:155], a[56:59], v[36:39]\n"
      "v_add_f32_e32 v15, v31, v15\n"
      "v_cvt_pk_bf16_f32 v43, v30, v31\n"
      "v_mfma_f32_16x16x32_bf16 v[36:39], a[156:159], a[60:63], v[36:39]\n"
      "v_exp_f32_e32 v32, v32\n"
      "v_mfma_f32_16x16x32_bf16 v[128:131], a[192:195], v[40:43], v[128:131]\n"
      "ds_read_b64_tr_b16 a[178:179], v3 offset:5120\n"
      "v_add_f32_e32 v16, v32, v16\n"
      "v_mfma_f32_16x16x32_bf16 v[132:135], a[196:199], v[40:43], v[132:135]\n"
      "v_exp_f32_e32 v33, v33\n"
      "v_mfma_f32_16x16x32_bf16 v[136:139], a[200:203], v[40:43], v[136:139]\n"
      "v_add_f32_e32 v16, v33, v16\n"
      "v_cvt_pk_bf16_f32 v44, v32, v33\n"
      "v_mfma_f32_16x16x32_bf16 v[140:143], a[204:207], v[40:43], v[140:143]\n"
      "v_exp_f32_e32 v34, v34\n"
      "v_mfma_f32_16x16x32_bf16 v[144:147], a[208:211], v[40:43], v[144:147]\n"
      "ds_read_b64_tr_b16 a[180:181], v4 offset:1024\n"
      "v_add_f32_e32 v16, v34, v16\n"
      "v_mfma_f32_16x16x32_bf16 v[148:151], a[212:215], v[40:43], v[148:151]\n"
      "v_exp_f32_e32 v35, v35\n"
      "v_mfma_f32_16x16x32_bf16 v[152:155], a[216:219], v[40:43], v[152:155]\n"
      "v_add_f32_e32 v16, v35, v16\n"
      "v_cvt_pk_bf16_f32 v45, v34, v35\n"
      "v_mfma_f32_16x16x32_bf16 v[156:159], a[220:223], v[40:43], v[156:159]\n"
      "v_exp_f32_e32 v36, v36\n"
      "v_mfma_f32_16x16x32_bf16 v[24:27], a[128:131], a[64:67], v[64:67]\n"
      "ds_read_b128 a[96:99], v1 offset:0\n"
      "v_add_f32_e32 v16, v36, v16\n"
      "v_mfma_f32_16x16x32_bf16 v[24:27], a[132:135], a[68:71], v[24:27]\n"
      "v_exp_f32_e32 v37, v37\n"
      "v_mfma_f32_16x16x32_bf16 v[24:27], a[136:139], a[72:75], v[24:27]\n"
      "v_add_f32_e32 v16, v37, v16\n"
      "v_cvt_pk_bf16_f32 v46, v36, v37\n"
      "v_mfma_f32_16x16x32_bf16 v[24:27], a[140:143], a[76:79], v[24:27]\n"
      "v_exp_f32_e32 v38, v38\n"
      "v_mfma_f32_16x16x32_bf16 v[28:31], a[144:147], a[64:67], v[64:67]\n"
      "ds_read_b128 a[100:103], v2 offset:0\n"
      "v_add_f32_e32 v16, v38, v16\n"
      "v_mfma_f32_16x16x32_bf16 v[28:31], a[148:151], a[68:71], v[28:31]\n"
      "v_exp_f32_e32 v39, v39\n"
      "v_mfma_f32_16x16x32_bf16 v[28:31], a[152:155], a[72:75], v[28:31]\n"
      "v_add_f32_e32 v16, v39, v16\n"
      "v_cvt_pk_bf16_f32 v47, v38, v39\n"
      "v_mfma_f32_16x16x32_bf16 v[28:31], a[156:159], a[76:79], v[28:31]\n"
      "v_exp_f32_e32 v24, v24\n"
      "v_mfma_f32_16x16x32_bf16 v[160:163], a[192:195], v[44:47], v[160:163]\n"
      "ds_read_b128 a[104:107], v1 offset:1024\n"
      "v_add_f32_e32 v17, v24, v17\n"
      "v_mfma_f32_16x16x32_bf16 v[164:167], a[196:199], v[44:47], v[164:167]\n"
      "v_exp_f32_e32 v25, v25\n"
      "v_mfma_f32_16x16x32_bf16 v[168:171], a[200:203], v[44:47], v[168:171]\n"
      "v_add_f32_e32 v17, v25, v17\n"
      "v_cvt_pk_bf16_f32 v40, v24, v25\n"
      "v_mfma_f32_16x16x32_bf16 v[172:175], a[204:207], v[44:47], v[172:175]\n"
      "v_exp_f32_e32 v26, v26\n"
      "v_mfma_f32_16x16x32_bf16 v[176:179], a[208:211], v[44:47], v[176:179]\n"
      "ds_read_b128 a[108:111], v2 offset:1024\n"
      "v_add_f32_e32 v17, v26, v17\n"
      "v_mfma_f32_16x16x32_bf16 v[180:183], a[212:215], v[44:47], v[180:183]\n"
      "v_exp_f32_e32 v27, v27\n"
      "v_mfma_f32_16x16x32_bf16 v[184:187], a[216:219], v[44:47], v[184:187]\n"
      "v_add_f32_e32 v17, v27, v17\n"
      "v_cvt_pk_bf16_f32 v41, v26, v27\n"
      "v_mfma_f32_16x16x32_bf16 v[188:191], a[220:223], v[44:47], v[188:191]\n"
      "v_exp_f32_e32 v28, v28\n"
      "v_mfma_f32_16x16x32_bf16 v[32:35], a[128:131], a[80:83], v[68:71]\n"
      "ds_read_b128 a[112:115], v1 offset:4096\n"
      "v_add_f32_e32 v17, v28, v17\n"
      "v_mfma_f32_16x16x32_bf16 v[32:35], a[132:135], a[84:87], v[32:35]\n"
      "v_exp_f32_e32 v29, v29\n"
      "v_mfma_f32_16x16x32_bf16 v[32:35], a[136:139], a[88:91], v[32:35]\n"
      "v_add_f32_e32 v17, v29, v17\n"
      "v_cvt_pk_bf16_f32 v42, v28, v29\n"
      "v_mfma_f32_16x16x32_bf16 v[32:35], a[140:143], a[92:95], v[32:35]\n"
      "v_exp_f32_e32 v30, v30\n"
      "v_mfma_f32_16x16x32_bf16 v[36:39], a[144:147], a[80:83], v[68:71]\n"
      "ds_read_b128 a[116:119], v2 offset:4096\n"
      "v_add_f32_e32 v17, v30, v17\n"
      "v_mfma_f32_16x16x32_bf16 v[36:39], a[148:151], a[84:87], v[36:39]\n"
      "v_exp_f32_e32 v31, v31\n"
      "v_mfma_f32_16x16x32_bf16 v[36:39], a[152:155], a[88:91], v[36:39]\n"
      "v_add_f32_e32 v17, v31, v17\n"
      "v_cvt_pk_bf16_f32 v43, v30, v31\n"
      "v_mfma_f32_16x16x32_bf16 v[36:39], a[156:159], a[92:95], v[36:39]\n"
      "v_exp_f32_e32 v32, v32\n"
      "v_mfma_f32_16x16x32_bf16 v[192:195], a[192:195], v[40:43], v[192:195]\n"
      "ds_read_b128 a[120:123], v1 offset:5120\n"
      "v_add_f32_e32 v18, v32, v18\n"
      "v_mfma_f32_16x16x32_bf16 v[196:199], a[196:199], v[40:43], v[196:199]\n"
      "v_exp_f32_e32 v33, v33\n"
      "v_mfma_f32_16x16x32_bf16 v[200:203], a[200:203], v[40:43], v[200:203]\n"
      "v_add_f32_e32 v18, v33, v18\n"
      "v_cvt_pk_bf16_f32 v44, v32, v33\n"
      "v_mfma_f32_16x16x32_bf16 v[204:207], a[204:207], v[40:43], v[204:207]\n"
      "v_exp_f32_e32 v34, v34\n"
      "v_mfma_f32_16x16x32_bf16 v[208:211], a[208:211], v[40:43], v[208:211]\n"
      "ds_read_b128 a[124:127], v2 offset:5120\n"
      "v_add_f32_e32 v18, v34, v18\n"
      "v_mfma_f32_16x16x32_bf16 v[212:215], a[212:215], v[40:43], v[212:215]\n"
      "v_exp_f32_e32 v35, v35\n"
      "v_mfma_f32_16x16x32_bf16 v[216:219], a[216:219], v[40:43], v[216:219]\n"
      "v_add_f32_e32 v18, v35, v18\n"
      "v_cvt_pk_bf16_f32 v45, v34, v35\n"
      "v_mfma_f32_16x16x32_bf16 v[220:223], a[220:223], v[40:43], v[220:223]\n"
      "v_exp_f32_e32 v36, v36\n"
      "s_waitcnt lgkmcnt(1)\n"
      "v_mfma_f32_16x16x32_bf16 v[24:27], a[96:99], a[0:3], v[48:51]\n"
      "ds_read_b64_tr_b16 a[182:183], v4 offset:5120\n"
      "v_add_f32_e32 v18, v36, v18\n"
      "v_mfma_f32_16x16x32_bf16 v[24:27], a[100:103], a[4:7], v[24:27]\n"
      "v_exp_f32_e32 v37, v37\n"
      "v_mfma_f32_16x16x32_bf16 v[24:27], a[104:107], a[8:11], v[24:27]\n"
      "v_add_f32_e32 v18, v37, v18\n"
      "v_cvt_pk_bf16_f32 v46, v36, v37\n"
      "v_mfma_f32_16x16x32_bf16 v[24:27], a[108:111], a[12:15], v[24:27]\n"
      "v_exp_f32_e32 v38, v38\n"
      "v_mfma_f32_16x16x32_bf16 v[28:31], a[112:115], a[0:3], v[48:51]\n"
      "ds_read_b64_tr_b16 a[184:185], v5 offset:1024\n"
      "v_add_f32_e32 v18, v38, v18\n"
      "v_mfma_f32_16x16x32_bf16 v[28:31], a[116:119], a[4:7], v[28:31]\n"
      "v_exp_f32_e32 v39, v39\n"
      "v_mfma_f32_16x16x32_bf16 v[28:31], a[120:123], a[8:11], v[28:31]\n"
      "v_add_f32_e32 v18, v39, v18\n"
      "v_cvt_pk_bf16_f32 v47, v38, v39\n"
      "s_waitcnt lgkmcnt(1)\n"
      "v_mfma_f32_16x16x32_bf16 v[28:31], a[124:127], a[12:15], v[28:31]\n"
      "v_exp_f32_e32 v24, v24\n"
      "v_mfma_f32_16x16x32_bf16 v[224:227], a[192:195], v[44:47], v[224:227]\n"
      "ds_read_b64_tr_b16 a[186:187], v5 offset:5120\n"
      "v_add_f32_e32 v13, v24, v13\n"
      "v_mfma_f32_16x16x32_bf16 v[228:231], a[196:199], v[44:47], v[228:231]\n"
      "v_exp_f32_e32 v25, v25\n"
      "v_mfma_f32_16x16x32_bf16 v[232:235], a[200:203], v[44:47], v[232:235]\n"
      "v_add_f32_e32 v13, v25, v13\n"
      "v_cvt_pk_bf16_f32 v40, v24, v25\n"
      "v_mfma_f32_16x16x32_bf16 v[236:239], a[204:207], v[44:47], v[236:239]\n"
      "s_waitcnt vmcnt(2)\n"
      "s_barrier\n"
      "v_exp_f32_e32 v26, v26\n"
      "v_mfma_f32_16x16x32_bf16 v[240:243], a[208:211], v[44:47], v[240:243]\n"
      "ds_read_b64_tr_b16 a[188:189], v6 offset:1024\n"
      "ds_read_b128 a[128:131], v1 offset:8192\n"
      "v_add_f32_e32 v13, v26, v13\n"
      "v_mfma_f32_16x16x32_bf16 v[244:247], a[212:215], v[44:47], v[244:247]\n"
      "v_exp_f32_e32 v27, v27\n"
      "v_mfma_f32_16x16x32_bf16 v[248:251], a[216:219], v[44:47], v[248:251]\n"
      "v_add_f32_e32 v13, v27, v13\n"
      "v_cvt_pk_bf16_f32 v41, v26, v27\n"
      "v_mfma_f32_16x16x32_bf16 v[252:255], a[220:223], v[44:47], v[252:255]\n"
      "v_exp_f32_e32 v28, v28\n"
      "v_mfma_f32_16x16x32_bf16 v[32:35], a[96:99], a[16:19], v[52:55]\n"
      "ds_read_b64_tr_b16 a[190:191], v6 offset:5120\n"
      "ds_read_b128 a[132:135], v2 offset:8192\n"
      "v_add_f32_e32 v13, v28, v13\n"
      "v_mfma_f32_16x16x32_bf16 v[32:35], a[100:103], a[20:23], v[32:35]\n"
      "v_exp_f32_e32 v29, v29\n"
      "v_mfma_f32_16x16x32_bf16 v[32:35], a[104:107], a[24:27], v[32:35]\n"
      "v_add_f32_e32 v13, v29, v13\n"
      "v_cvt_pk_bf16_f32 v42, v28, v29\n"
      "v_mfma_f32_16x16x32_bf16 v[32:35], a[108:111], a[28:31], v[32:35]\n"
      "v_exp_f32_e32 v30, v30\n"
      "v_mfma_f32_16x16x32_bf16 v[36:39], a[112:115], a[16:19], v[52:55]\n"
      "ds_read_b128 a[136:139], v1 offset:9216\n"
      "ds_read_b128 a[140:143], v2 offset:9216\n"
      "v_add_f32_e32 v13, v30, v13\n"
      "v_mfma_f32_16x16x32_bf16 v[36:39], a[116:119], a[20:23], v[36:39]\n"
      "v_exp_f32_e32 v31, v31\n"
      "v_mfma_f32_16x16x32_bf16 v[36:39], a[120:123], a[24:27], v[36:39]\n"
      "v_add_f32_e32 v13, v31, v13\n"
      "v_cvt_pk_bf16_f32 v43, v30, v31\n"
      "v_mfma_f32_16x16x32_bf16 v[36:39], a[124:127], a[28:31], v[36:39]\n"
      "v_exp_f32_e32 v32, v32\n"
      "v_mfma_f32_16x16x32_bf16 a[224:227], a[160:163], v[40:43], a[224:227]\n"
      "ds_read_b128 a[144:147], v1 offset:12288\n"
      "ds_read_b128 a[148:151], v2 offset:12288\n"
      "v_add_f32_e32 v14, v32, v14\n"
      "v_mfma_f32_16x16x32_bf16 a[228:231], a[164:167], v[40:43], a[228:231]\n"
      "v_exp_f32_e32 v33, v33\n"
      "v_mfma_f32_16x16x32_bf16 a[232:235], a[168:171], v[40:43], a[232:235]\n"
      "v_add_f32_e32 v14, v33, v14\n"
      "v_cvt_pk_bf16_f32 v44, v32, v33\n"
      "v_mfma_f32_16x16x32_bf16 a[236:239], a[172:175], v[40:43], a[236:239]\n"
      "v_exp_f32_e32 v34, v34\n"
      "v_mfma_f32_16x16x32_bf16 a[240:243], a[176:179], v[40:43], a[240:243]\n"
      "ds_read_b128 a[152:155], v1 offset:13312\n"
      "ds_read_b128 a[156:159], v2 offset:13312\n"
      "v_add_f32_e32 v14, v34, v14\n"
      "v_mfma_f32_16x16x32_bf16 a[244:247], a[180:183], v[40:43], a[244:247]\n"
      "v_exp_f32_e32 v35, v35\n"
      "s_waitcnt lgkmcnt(2)\n"
      "v_mfma_f32_16x16x32_bf16 a[248:251], a[184:187], v[40:43], a[248:251]\n"
      "v_add_f32_e32 v14, v35, v14\n"
      "v_cvt_pk_bf16_f32 v45, v34, v35\n"
      "v_mfma_f32_16x16x32_bf16 a[252:255], a[188:191], v[40:43], a[252:255]\n"
      "v_exp_f32_e32 v36, v36\n"
      "v_mfma_f32_16x16x32_bf16 v[24:27], a[128:131], a[0:3], v[48:51]\n"
      "ds_read_b64_tr_b16 a[192:193], v3 offset:8192\n"
      "ds_read_b64_tr_b16 a[194:195], v3 offset:12288\n"
      "v_add_f32_e32 v14, v36, v14\n"
      "v_mfma_f32_16x16x32_bf16 v[24:27], a[132:135], a[4:7], v[24:27]\n"
      "v_exp_f32_e32 v37, v37\n"
      "v_mfma_f32_16x16x32_bf16 v[24:27], a[136:139], a[8:11], v[24:27]\n"
      "v_add_f32_e32 v14, v37, v14\n"
      "v_cvt_pk_bf16_f32 v46, v36, v37\n"
      "v_mfma_f32_16x16x32_bf16 v[24:27], a[140:143], a[12:15], v[24:27]\n"
      "v_exp_f32_e32 v38, v38\n"
      "v_mfma_f32_16x16x32_bf16 v[28:31], a[144:147], a[0:3], v[48:51]\n"
      "ds_read_b64_tr_b16 a[196:197], v4 offset:8192\n"
      "ds_read_b64_tr_b16 a[198:199], v4 offset:12288\n"
      "v_add_f32_e32 v14, v38, v14\n"
      "v_mfma_f32_16x16x32_bf16 v[28:31], a[148:151], a[4:7], v[28:31]\n"
      "v_exp_f32_e32 v39, v39\n"
      "s_waitcnt lgkmcnt(2)\n"
      "v_mfma_f32_16x16x32_bf16 v[28:31], a[152:155], a[8:11], v[28:31]\n"
      "v_add_f32_e32 v14, v39, v14\n"
      "v_cvt_pk_bf16_f32 v47, v38, v39\n"
      "v_mfma_f32_16x16x32_bf16 v[28:31], a[156:159], a[12:15], v[28:31]\n"
      "v_exp_f32_e32 v24, v24\n"
      "v_mfma_f32_16x16x32_bf16 v[96:99], a[160:163], v[44:47], v[96:99]\n"
      "ds_read_b64_tr_b16 a[200:201], v5 offset:8192\n"
      "ds_read_b64_tr_b16 a[202:203], v5 offset:12288\n"
      "v_add_f32_e32 v13, v24, v13\n"
      "v_mfma_f32_16x16x32_bf16 v[100:103], a[164:167], v[44:47], v[100:103]\n"
      "ds_read_b64_tr_b16 a[204:205], v6 offset:8192\n"
      "v_exp_f32_e32 v25, v25\n"
      "v_mfma_f32_16x16x32_bf16 v[104:107], a[168:171], v[44:47], v[104:107]\n"
      "v_add_f32_e32 v13, v25, v13\n"
      "v_cvt_pk_bf16_f32 v40, v24, v25\n"
      "v_mfma_f32_16x16x32_bf16 v[108:111], a[172:175], v[44:47], v[108:111]\n"
      "ds_read_b64_tr_b16 a[206:207], v6 offset:12288\n"
      "v_exp_f32_e32 v26, v26\n"
      "v_mfma_f32_16x16x32_bf16 v[112:115], a[176:179], v[44:47], v[112:115]\n"
      "ds_read_b64_tr_b16 a[208:209], v3 offset:9216\n"
      "ds_read_b64_tr_b16 a[210:211], v3 offset:13312\n"
      "v_add_f32_e32 v13, v26, v13\n"
      "v_mfma_f32_16x16x32_bf16 v[116:119], a[180:183], v[44:47], v[116:119]\n"
      "ds_read_b64_tr_b16 a[212:213], v4 offset:9216\n"
      "v_exp_f32_e32 v27, v27\n"
      "v_mfma_f32_16x16x32_bf16 v[120:123], a[184:187], v[44:47], v[120:123]\n"
      "v_add_f32_e32 v13, v27, v13\n"
      "v_cvt_pk_bf16_f32 v41, v26, v27\n"
      "v_mfma_f32_16x16x32_bf16 v[124:127], a[188:191], v[44:47], v[124:127]\n"
      "ds_read_b64_tr_b16 a[214:215], v4 offset:13312\n"
      "v_exp_f32_e32 v28, v28\n"
      "v_mfma_f32_16x16x32_bf16 v[32:35], a[128:131], a[16:19], v[52:55]\n"
      "ds_read_b64_tr_b16 a[216:217], v5 offset:9216\n"
      "ds_read_b64_tr_b16 a[218:219], v5 offset:13312\n"
      "v_add_f32_e32 v13, v28, v13\n"
      "v_mfma_f32_16x16x32_bf16 v[32:35], a[132:135], a[20:23], v[32:35]\n"
      "ds_read_b64_tr_b16 a[220:221], v6 offset:9216\n"
      "v_exp_f32_e32 v29, v29\n"
      "v_mfma_f32_16x16x32_bf16 v[32:35], a[136:139], a[24:27], v[32:35]\n"
      "v_add_f32_e32 v13, v29, v13\n"
      "v_cvt_pk_bf16_f32 v42, v28, v29\n"
      "v_mfma_f32_16x16x32_bf16 v[32:35], a[140:143], a[28:31], v[32:35]\n"
      "ds_read_b64_tr_b16 a[222:223], v6 offset:13312\n"
      "v_exp_f32_e32 v30, v30\n"
      "v_mfma_f32_16x16x32_bf16 v[36:39], a[144:147], a[16:19], v[52:55]\n"
      "ds_read_b128 a[96:99], v1 offset:16384\n"
      "ds_read_b128 a[100:103], v2 offset:16384\n"
      "v_add_f32_e32 v13, v30, v13\n"
      "v_mfma_f32_16x16x32_bf16 v[36:39], a[148:151], a[20:23], v[36:39]\n"
      "v_exp_f32_e32 v31, v31\n"
      "v_mfma_f32_16x16x32_bf16 v[36:39], a[152:155], a[24:27], v[36:39]\n"
      "v_add_f32_e32 v13, v31, v13\n"
      "v_cvt_pk_bf16_f32 v43, v30, v31\n"
      "v_mfma_f32_16x16x32_bf16 v[36:39], a[156:159], a[28:31], v[36:39]\n"
      "v_exp_f32_e32 v32, v32\n"
      "v_mfma_f32_16x16x32_bf16 a[224:227], a[192:195], v[40:43], a[224:227]\n"
      "ds_read_b128 a[104:107], v1 offset:17408\n"
      "ds_read_b128 a[108:111], v2 offset:17408\n"
      "v_add_f32_e32 v14, v32, v14\n"
      "s_waitcnt lgkmcnt(2)\n"
      "v_mfma_f32_16x16x32_bf16 a[228:231], a[196:199], v[40:43], a[228:231]\n"
      "v_exp_f32_e32 v33, v33\n"
      "v_mfma_f32_16x16x32_bf16 a[232:235], a[200:203], v[40:43], a[232:235]\n"
      "v_add_f32_e32 v14, v33, v14\n"
      "v_cvt_pk_bf16_f32 v44, v32, v33\n"
      "v_mfma_f32_16x16x32_bf16 a[236:239], a[204:207], v[40:43], a[236:239]\n"
      "ds_read_b128 a[112:115], v1 offset:20480\n"
      "v_exp_f32_e32 v34, v34\n"
      "v_mfma_f32_16x16x32_bf16 a[240:243], a[208:211], v[40:43], a[240:243]\n"
      "ds_read_b128 a[116:119], v2 offset:20480\n"
      "ds_read_b128 a[120:123], v1 offset:21504\n"
      "v_add_f32_e32 v14, v34, v14\n"
      "v_mfma_f32_16x16x32_bf16 a[244:247], a[212:215], v[40:43], a[244:247]\n"
      "ds_read_b128 a[124:127], v2 offset:21504\n"
      "v_exp_f32_e32 v35, v35\n"
      "v_mfma_f32_16x16x32_bf16 a[248:251], a[216:219], v[40:43], a[248:251]\n"
      "v_add_f32_e32 v14, v35, v14\n"
      "v_cvt_pk_bf16_f32 v45, v34, v35\n"
      "v_mfma_f32_16x16x32_bf16 a[252:255], a[220:223], v[40:43], a[252:255]\n"
      "v_exp_f32_e32 v36, v36\n"
      "v_mfma_f32_16x16x32_bf16 v[24:27], a[96:99], a[0:3], v[48:51]\n"
      "ds_read_b64_tr_b16 a[160:161], v3 offset:16384\n"
      "ds_read_b64_tr_b16 a[162:163], v3 offset:20480\n"
      "v_add_f32_e32 v14, v36, v14\n"
      "v_mfma_f32_16x16x32_bf16 v[24:27], a[100:103], a[4:7], v[24:27]\n"
      "v_exp_f32_e32 v37, v37\n"
      "s_waitcnt lgkmcnt(2)\n"
      "v_mfma_f32_16x16x32_bf16 v[24:27], a[104:107], a[8:11], v[24:27]\n"
      "v_add_f32_e32 v14, v37, v14\n"
      "v_cvt_pk_bf16_f32 v46, v36, v37\n"
      "v_mfma_f32_16x16x32_bf16 v[24:27], a[108:111], a[12:15], v[24:27]\n"
      "v_exp_f32_e32 v38, v38\n"
      "v_mfma_f32_16x16x32_bf16 v[28:31], a[112:115], a[0:3], v[48:51]\n"
      "ds_read_b64_tr_b16 a[164:165], v4 offset:16384\n"
      "ds_read_b64_tr_b16 a[166:167], v4 offset:20480\n"
      "v_add_f32_e32 v14, v38, v14\n"
      "v_mfma_f32_16x16x32_bf16 v[28:31], a[116:119], a[4:7], v[28:31]\n"
      "v_exp_f32_e32 v39, v39\n"
      "v_mfma_f32_16x16x32_bf16 v[28:31], a[120:123], a[8:11], v[28:31]\n"
      "v_add_f32_e32 v14, v39, v14\n"
      "v_cvt_pk_bf16_f32 v47, v38, v39\n"
      "v_mfma_f32_16x16x32_bf16 v[28:31], a[124:127], a[12:15], v[28:31]\n"
      "v_exp_f32_e32 v24, v24\n"
      "v_mfma_f32_16x16x32_bf16 v[96:99], a[192:195], v[44:47], v[96:99]\n"
      "ds_read_b64_tr_b16 a[168:169], v5 offset:16384\n"
      "ds_read_b64_tr_b16 a[170:171], v5 offset:20480\n"
      "v_add_f32_e32 v13, v24, v13\n"
      "v_mfma_f32_16x16x32_bf16 v[100:103], a[196:199], v[44:47], v[100:103]\n"
      "ds_read_b64_tr_b16 a[172:173], v6 offset:16384\n"
      "v_exp_f32_e32 v25, v25\n"
      "v_mfma_f32_16x16x32_bf16 v[104:107], a[200:203], v[44:47], v[104:107]\n"
      "v_add_f32_e32 v13, v25, v13\n"
      "v_cvt_pk_bf16_f32 v40, v24, v25\n"
      "v_mfma_f32_16x16x32_bf16 v[108:111], a[204:207], v[44:47], v[108:111]\n"
      "ds_read_b64_tr_b16 a[174:175], v6 offset:20480\n"
      "v_exp_f32_e32 v26, v26\n"
      "v_mfma_f32_16x16x32_bf16 v[112:115], a[208:211], v[44:47], v[112:115]\n"
      "ds_read_b64_tr_b16 a[176:177], v3 offset:17408\n"
      "ds_read_b64_tr_b16 a[178:179], v3 offset:21504\n"
      "v_add_f32_e32 v13, v26, v13\n"
      "v_mfma_f32_16x16x32_bf16 v[116:119], a[212:215], v[44:47], v[116:119]\n"
      "ds_read_b64_tr_b16 a[180:181], v4 offset:17408\n"
      "v_exp_f32_e32 v27, v27\n"
      "v_mfma_f32_16x16x32_bf16 v[120:123], a[216:219], v[44:47], v[120:123]\n"
      "v_add_f32_e32 v13, v27, v13\n"
      "v_cvt_pk_bf16_f32 v41, v26, v27\n"
      "v_mfma_f32_16x16x32_bf16 v[124:127], a[220:223], v[44:47], v[124:127]\n"
      "ds_read_b64_tr_b16 a[182:183], v4 offset:21504\n"
      "v_exp_f32_e32 v28, v28\n"
      "v_mfma_f32_16x16x32_bf16 v[32:35], a[96:99], a[16:19], v[52:55]\n"
      "ds_read_b64_tr_b16 a[184:185], v5 offset:17408\n"
      "ds_read_b64_tr_b16 a[186:187], v5 offset:21504\n"
      "v_add_f32_e32 v13, v28, v13\n"
      "v_mfma_f32_16x16x32_bf16 v[32:35], a[100:103], a[20:23], v[32:35]\n"
      "ds_read_b64_tr_b16 a[188:189], v6 offset:17408\n"
      "v_exp_f32_e32 v29, v29\n"
      "v_mfma_f32_16x16x32_bf16 v[32:35], a[104:107], a[24:27], v[32:35]\n"
      "v_add_f32_e32 v13, v29, v13\n"
      "v_cvt_pk_bf16_f32 v42, v28, v29\n"
      "v_mfma_f32_16x16x32_bf16 v[32:35], a[108:111], a[28:31], v[32:35]\n"
      "ds_read_b64_tr_b16 a[190:191], v6 offset:21504\n"
      "v_exp_f32_e32 v30, v30\n"
      "v_mfma_f32_16x16x32_bf16 v[36:39], a[112:115], a[16:19], v[52:55]\n"
      "ds_read_b128 a[128:131], v1 offset:24576\n"
      "ds_read_b128 a[132:135], v2 offset:24576\n"
      "v_add_f32_e32 v13, v30, v13\n"
      "v_mfma_f32_16x16x32_bf16 v[36:39], a[116:119], a[20:23], v[36:39]\n"
      "v_exp_f32_e32 v31, v31\n"
      "v_mfma_f32_16x16x32_bf16 v[36:39], a[120:123], a[24:27], v[36:39]\n"
      "v_add_f32_e32 v13, v31, v13\n"
      "v_cvt_pk_bf16_f32 v43, v30, v31\n"
      "v_mfma_f32_16x16x32_bf16 v[36:39], a[124:127], a[28:31], v[36:39]\n"
      "v_exp_f32_e32 v32, v32\n"
      "s_waitcnt lgkmcnt(2)\n"
      "v_mfma_f32_16x16x32_bf16 a[224:227], a[160:163], v[40:43], a[224:227]\n"
      "ds_read_b128 a[136:139], v1 offset:25600\n"
      "ds_read_b128 a[140:143], v2 offset:25600\n"
      "v_add_f32_e32 v14, v32, v14\n"
      "v_mfma_f32_16x16x32_bf16 a[228:231], a[164:167], v[40:43], a[228:231]\n"
      "v_exp_f32_e32 v33, v33\n"
      "v_mfma_f32_16x16x32_bf16 a[232:235], a[168:171], v[40:43], a[232:235]\n"
      "v_add_f32_e32 v14, v33, v14\n"
      "v_cvt_pk_bf16_f32 v44, v32, v33\n"
      "v_mfma_f32_16x16x32_bf16 a[236:239], a[172:175], v[40:43], a[236:239]\n"
      "ds_read_b128 a[144:147], v1 offset:28672\n"
      "v_exp_f32_e32 v34, v34\n"
      "v_mfma_f32_16x16x32_bf16 a[240:243], a[176:179], v[40:43], a[240:243]\n"
      "ds_read_b128 a[148:151], v2 offset:28672\n"
      "ds_read_b128 a[152:155], v1 offset:29696\n"
      "v_add_f32_e32 v14, v34, v14\n"
      "v_mfma_f32_16x16x32_bf16 a[244:247], a[180:183], v[40:43], a[244:247]\n"
      "ds_read_b128 a[156:159], v2 offset:29696\n"
      "v_exp_f32_e32 v35, v35\n"
      "v_mfma_f32_16x16x32_bf16 a[248:251], a[184:187], v[40:43], a[248:251]\n"
      "v_add_f32_e32 v14, v35, v14\n"
      "v_cvt_pk_bf16_f32 v45, v34, v35\n"
      "v_mfma_f32_16x16x32_bf16 a[252:255], a[188:191], v[40:43], a[252:255]\n"
      "s_waitcnt vmcnt(0)\n"
      "s_barrier\n"
      "v_exp_f32_e32 v36, v36\n"
      "s_waitcnt lgkmcnt(3)\n"
      "v_mfma_f32_16x16x32_bf16 v[24:27], a[128:131], a[0:3], v[48:51]\n"
      "ds_read_b64_tr_b16 a[192:193], v3 offset:24576\n"
      "ds_read_b64_tr_b16 a[194:195], v3 offset:28672\n"
      "v_add_f32_e32 v14, v36, v14\n"
      "v_mfma_f32_16x16x32_bf16 v[24:27], a[132:135], a[4:7], v[24:27]\n"
      "v_exp_f32_e32 v37, v37\n"
      "v_mfma_f32_16x16x32_bf16 v[24:27], a[136:139], a[8:11], v[24:27]\n"
      "v_add_f32_e32 v14, v37, v14\n"
      "v_cvt_pk_bf16_f32 v46, v36, v37\n"
      "v_mfma_f32_16x16x32_bf16 v[24:27], a[140:143], a[12:15], v[24:27]\n"
      "v_exp_f32_e32 v38, v38\n"
      "v_mfma_f32_16x16x32_bf16 v[28:31], a[144:147], a[0:3], v[48:51]\n"
      "ds_read_b64_tr_b16 a[196:197], v4 offset:24576\n"
      "ds_read_b64_tr_b16 a[198:199], v4 offset:28672\n"
      "v_add_f32_e32 v14, v38, v14\n"
      "s_waitcnt lgkmcnt(2)\n"
      "v_mfma_f32_16x16x32_bf16 v[28:31], a[148:151], a[4:7], v[28:31]\n"
      "v_exp_f32_e32 v39, v39\n"
      "v_mfma_f32_16x16x32_bf16 v[28:31], a[152:155], a[8:11], v[28:31]\n"
      "v_add_f32_e32 v14, v39, v14\n"
      "v_cvt_pk_bf16_f32 v47, v38, v39\n"
      "v_mfma_f32_16x16x32_bf16 v[28:31], a[156:159], a[12:15], v[28:31]\n"
      "v_exp_f32_e32 v24, v24\n"
      "v_mfma_f32_16x16x32_bf16 v[96:99], a[160:163], v[44:47], v[96:99]\n"
      "ds_read_b64_tr_b16 a[200:201], v5 offset:24576\n"
      "ds_read_b64_tr_b16 a[202:203], v5 offset:28672\n"
      "v_add_f32_e32 v13, v24, v13\n"
      "v_mfma_f32_16x16x32_bf16 v[100:103], a[164:167], v[44:47], v[100:103]\n"
      "ds_read_b64_tr_b16 a[204:205], v6 offset:24576\n"
      "v_exp_f32_e32 v25, v25\n"
      "v_mfma_f32_16x16x32_bf16 v[104:107], a[168:171], v[44:47], v[104:107]\n"
      "v_add_f32_e32 v13, v25, v13\n"
      "v_cvt_pk_bf16_f32 v40, v24, v25\n"
      "v_mfma_f32_16x16x32_bf16 v[108:111], a[172:175], v[44:47], v[108:111]\n"
      "ds_read_b64_tr_b16 a[206:207], v6 offset:28672\n"
      "v_exp_f32_e32 v26, v26\n"
      "v_mfma_f32_16x16x32_bf16 v[112:115], a[176:179], v[44:47], v[112:115]\n"
      "ds_read_b64_tr_b16 a[208:209], v3 offset:25600\n"
      "ds_read_b64_tr_b16 a[210:211], v3 offset:29696\n"
      "v_add_f32_e32 v13, v26, v13\n"
      "v_mfma_f32_16x16x32_bf16 v[116:119], a[180:183], v[44:47], v[116:119]\n"
      "ds_read_b64_tr_b16 a[212:213], v4 offset:25600\n"
      "v_exp_f32_e32 v27, v27\n"
      "v_mfma_f32_16x16x32_bf16 v[120:123], a[184:187], v[44:47], v[120:123]\n"
      "v_add_f32_e32 v13, v27, v13\n"
      "v_cvt_pk_bf16_f32 v41, v26, v27\n"
      "v_mfma_f32_16x16x32_bf16 v[124:127], a[188:191], v[44:47], v[124:127]\n"
      "ds_read_b64_tr_b16 a[214:215], v4 offset:29696\n"
      "v_exp_f32_e32 v28, v28\n"
      "v_mfma_f32_16x16x32_bf16 v[32:35], a[128:131], a[16:19], v[52:55]\n"
      "ds_read_b64_tr_b16 a[216:217], v5 offset:25600\n"
      "ds_read_b64_tr_b16 a[218:219], v5 offset:29696\n"
      "v_add_f32_e32 v13, v28, v13\n"
      "v_mfma_f32_16x16x32_bf16 v[32:35], a[132:135], a[20:23], v[32:35]\n"
      "ds_read_b64_tr_b16 a[220:221], v6 offset:25600\n"
      "v_exp_f32_e32 v29, v29\n"
      "v_mfma_f32_16x16x32_bf16 v[32:35], a[136:139], a[24:27], v[32:35]\n"
      "v_add_f32_e32 v13, v29, v13\n"
      "v_cvt_pk_bf16_f32 v42, v28, v29\n"
      "v_mfma_f32_16x16x32_bf16 v[32:35], a[140:143], a[28:31], v[32:35]\n"
      "ds_read_b64_tr_b16 a[222:223], v6 offset:29696\n"
      "v_exp_f32_e32 v30, v30\n"
      "v_mfma_f32_16x16x32_bf16 v[36:39], a[144:147], a[16:19], v[52:55]\n"
      "ds_read_b128 a[96:99], v1 offset:0\n"
      "ds_read_b128 a[100:103], v2 offset:0\n"
      "v_add_f32_e32 v13, v30, v13\n"
      "v_mfma_f32_16x16x32_bf16 v[36:39], a[148:151], a[20:23], v[36:39]\n"
      "v_exp_f32_e32 v31, v31\n"
      "v_mfma_f32_16x16x32_bf16 v[36:39], a[152:155], a[24:27], v[36:39]\n"
      "v_add_f32_e32 v13, v31, v13\n"
      "v_cvt_pk_bf16_f32 v43, v30, v31\n"
      "v_mfma_f32_16x16x32_bf16 v[36:39], a[156:159], a[28:31], v[36:39]\n"
      "v_exp_f32_e32 v32, v32\n"
      "v_mfma_f32_16x16x32_bf16 a[224:227], a[192:195], v[40:43], a[224:227]\n"
      "ds_read_b128 a[104:107], v1 offset:1024\n"
      "ds_read_b128 a[108:111], v2 offset:1024\n"
      "v_add_f32_e32 v14, v32, v14\n"
      "v_mov_b32_e32 v23, v13\n"
      "s_nop 1\n"
      "v_permlane16_swap_b32_e32 v23, v13\n"
      "s_waitcnt lgkmcnt(2)\n"
      "v_mfma_f32_16x16x32_bf16 a[228:231], a[196:199], v[40:43], a[228:231]\n"
      "v_exp_f32_e32 v33, v33\n"
      "v_add_f32_e32 v13, v23, v13\n"
      "v_mov_b32_e32 v23, v13\n"
      "v_mfma_f32_16x16x32_bf16 a[232:235], a[200:203], v[40:43], a[232:235]\n"
      "v_add_f32_e32 v14, v33, v14\n"
      "v_cvt_pk_bf16_f32 v44, v32, v33\n"
      "v_permlane32_swap_b32_e32 v23, v13\n"
      "v_add_f32_e32 v13, v23, v13\n"
      "v_mfma_f32_16x16x32_bf16 a[236:239], a[204:207], v[40:43], a[236:239]\n"
      "ds_read_b128 a[112:115], v1 offset:4096\n"
      "v_exp_f32_e32 v34, v34\n"
      "v_cmp_nge_f32_e32 vcc, s46, v13\n"
      "v_rcp_f32_e32 v13, v13\n"
      "v_mfma_f32_16x16x32_bf16 a[240:243], a[208:211], v[40:43], a[240:243]\n"
      "ds_read_b128 a[116:119], v2 offset:4096\n"
      "ds_read_b128 a[120:123], v1 offset:5120\n"
      "v_add_f32_e32 v14, v34, v14\n"
      "s_or_b64 s[44:45], s[44:45], vcc\n"
      "v_mfma_f32_16x16x32_bf16 a[244:247], a[212:215], v[40:43], a[244:247]\n"
      "ds_read_b128 a[124:127], v2 offset:5120\n"
      "v_exp_f32_e32 v35, v35\n"
      "v_accvgpr_read_b32 v72, a224\n"
      "v_accvgpr_read_b32 v73, a225\n"
      "v_accvgpr_read_b32 v74, a226\n"
      "v_mfma_f32_16x16x32_bf16 a[248:251], a[216:219], v[40:43], a[248:251]\n"
      "v_add_f32_e32 v14, v35, v14\n"
      "v_cvt_pk_bf16_f32 v45, v34, v35\n"
      "v_accvgpr_read_b32 v75, a227\n"
      "v_mul_f32_e32 v72, v13, v72\n"
      "v_mul_f32_e32 v73, v13, v73\n"
      "v_mfma_f32_16x16x32_bf16 a[252:255], a[220:223], v[40:43], a[252:255]\n"
      "v_exp_f32_e32 v36, v36\n"
      "v_mul_f32_e32 v74, v13, v74\n"
      "v_mul_f32_e32 v75, v13, v75\n"
      "v_cvt_pk_bf16_f32 v72, v72, v73\n"
      "v_mfma_f32_16x16x32_bf16 v[24:27], a[96:99], a[32:35], v[56:59]\n"
      "ds_read_b64_tr_b16 a[160:161], v3 offset:0\n"
      "ds_read_b64_tr_b16 a[162:163], v3 offset:4096\n"
      "v_add_f32_e32 v14, v36, v14\n"
      "v_cvt_pk_bf16_f32 v73, v74, v75\n"
      "ds_write_b64 v89, v[72:73] offset:0\n"
      "v_accvgpr_read_b32 v76, a228\n"
      "v_mfma_f32_16x16x32_bf16 v[24:27], a[100:103], a[36:39], v[24:27]\n"
      "v_exp_f32_e32 v37, v37\n"
      "v_accvgpr_read_b32 v77, a229\n"
      "v_accvgpr_read_b32 v78, a230\n"
      "v_accvgpr_read_b32 v79, a231\n"
      "s_waitcnt lgkmcnt(3)\n"
      "v_mfma_f32_16x16x32_bf16 v[24:27], a[104:107], a[40:43], v[24:27]\n"
      "v_add_f32_e32 v14, v37, v14\n"
      "v_cvt_pk_bf16_f32 v46, v36, v37\n"
      "v_mul_f32_e32 v76, v13, v76\n"
      "v_mul_f32_e32 v77, v13, v77\n"
      "v_mul_f32_e32 v78, v13, v78\n"
      "v_mfma_f32_16x16x32_bf16 v[24:27], a[108:111], a[44:47], v[24:27]\n"
      "v_exp_f32_e32 v38, v38\n"
      "v_mul_f32_e32 v79, v13, v79\n"
      "v_cvt_pk_bf16_f32 v76, v76, v77\n"
      "v_cvt_pk_bf16_f32 v77, v78, v79\n"
      "v_mfma_f32_16x16x32_bf16 v[28:31], a[112:115], a[32:35], v[56:59]\n"
      "ds_read_b64_tr_b16 a[164:165], v4 offset:0\n"
      "ds_read_b64_tr_b16 a[166:167], v4 offset:4096\n"
      "v_add_f32_e32 v14, v38, v14\n"
      "ds_write_b64 v89, v[76:77] offset:32\n"
      "v_accvgpr_read_b32 v80, a232\n"
      "v_accvgpr_read_b32 v81, a233\n"
      "v_mfma_f32_16x16x32_bf16 v[28:31], a[116:119], a[36:39], v[28:31]\n"
      "v_exp_f32_e32 v39, v39\n"
      "v_accvgpr_read_b32 v82, a234\n"
      "v_accvgpr_read_b32 v83, a235\n"
      "v_mul_f32_e32 v80, v13, v80\n"
      "v_mfma_f32_16x16x32_bf16 v[28:31], a[120:123], a[40:43], v[28:31]\n"
      "v_add_f32_e32 v14, v39, v14\n"
      "v_cvt_pk_bf16_f32 v47, v38, v39\n"
      "v_mul_f32_e32 v81, v13, v81\n"
      "v_mul_f32_e32 v82, v13, v82\n"
      "v_mul_f32_e32 v83, v13, v83\n"
      "v_mfma_f32_16x16x32_bf16 v[28:31], a[124:127], a[44:47], v[28:31]\n"
      "v_exp_f32_e32 v24, v24\n"
      "v_cvt_pk_bf16_f32 v80, v80, v81\n"
      "v_cvt_pk_bf16_f32 v81, v82, v83\n"
      "ds_write_b64 v89, v[80:81] offset:64\n"
      "v_mfma_f32_16x16x32_bf16 v[96:99], a[192:195], v[44:47], v[96:99]\n"
      "ds_read_b64_tr_b16 a[168:169], v5 offset:0\n"
      "ds_read_b64_tr_b16 a[170:171], v5 offset:4096\n"
      "v_add_f32_e32 v15, v24, v15\n"
      "v_accvgpr_read_b32 v84, a236\n"
      "v_accvgpr_read_b32 v85, a237\n"
      "v_accvgpr_read_b32 v86, a238\n"
      "v_mfma_f32_16x16x32_bf16 v[100:103], a[196:199], v[44:47], v[100:103]\n"
      "ds_read_b64_tr_b16 a[172:173], v6 offset:0\n"
      "v_exp_f32_e32 v25, v25\n"
      "v_accvgpr_read_b32 v87, a239\n"
      "v_mul_f32_e32 v84, v13, v84\n"
      "v_mul_f32_e32 v85, v13, v85\n"
      "v_mfma_f32_16x16x32_bf16 v[104:107], a[200:203], v[44:47], v[104:107]\n"
      "v_add_f32_e32 v15, v25, v15\n"
      "v_cvt_pk_bf16_f32 v40, v24, v25\n"
      "v_mul_f32_e32 v86, v13, v86\n"
      "v_mul_f32_e32 v87, v13, v87\n"
      "v_cvt_pk_bf16_f32 v84, v84, v85\n"
      "v_mfma_f32_16x16x32_bf16 v[108:111], a[204:207], v[44:47], v[108:111]\n"
      "ds_read_b64_tr_b16 a[174:175], v6 offset:4096\n"
      "v_exp_f32_e32 v26, v26\n"
      "v_cvt_pk_bf16_f32 v85, v86, v87\n"
      "ds_write_b64 v89, v[84:85] offset:96\n"
      "v_accvgpr_read_b32 v72, a240\n"
      "v_mfma_f32_16x16x32_bf16 v[112:115], a[208:211], v[44:47], v[112:115]\n"
      "ds_read_b64_tr_b16 a[176:177], v3 offset:1024\n"
      "ds_read_b64_tr_b16 a[178:179], v3 offset:5120\n"
      "v_add_f32_e32 v15, v26, v15\n"
      "v_accvgpr_read_b32 v73, a241\n"
      "v_accvgpr_read_b32 v74, a242\n"
      "v_accvgpr_read_b32 v75, a243\n"
      "v_mfma_f32_16x16x32_bf16 v[116:119], a[212:215], v[44:47], v[116:119]\n"
      "ds_read_b64_tr_b16 a[180:181], v4 offset:1024\n"
      "v_exp_f32_e32 v27, v27\n"
      "v_mul_f32_e32 v72, v13, v72\n"
      "v_mul_f32_e32 v73, v13, v73\n"
      "v_mul_f32_e32 v74, v13, v74\n"
      "v_mfma_f32_16x16x32_bf16 v[120:123], a[216:219], v[44:47], v[120:123]\n"
      "v_add_f32_e32 v15, v27, v15\n"
      "v_cvt_pk_bf16_f32 v41, v26, v27\n"
      "v_mul_f32_e32 v75, v13, v75\n"
      "v_cvt_pk_bf16_f32 v72, v72, v73\n"
      "v_cvt_pk_bf16_f32 v73, v74, v75\n"
      "v_mfma_f32_16x16x32_bf16 v[124:127], a[220:223], v[44:47], v[124:127]\n"
      "ds_read_b64_tr_b16 a[182:183], v4 offset:5120\n"
      "v_exp_f32_e32 v28, v28\n"
      "ds_write_b64 v89, v[72:73] offset:128\n"
      "v_accvgpr_read_b32 v76, a244\n"
      "v_accvgpr_read_b32 v77, a245\n"
      "v_mfma_f32_16x16x32_bf16 v[32:35], a[96:99], a[48:51], v[60:63]\n"
      "ds_read_b64_tr_b16 a[184:185], v5 offset:1024\n"
      "ds_read_b64_tr_b16 a[186:187], v5 offset:5120\n"
      "v_add_f32_e32 v15, v28, v15\n"
      "v_accvgpr_read_b32 v78, a246\n"
      "v_accvgpr_read_b32 v79, a247\n"
      "v_mul_f32_e32 v76, v13, v76\n"
      "v_mfma_f32_16x16x32_bf16 v[32:35], a[100:103], a[52:55], v[32:35]\n"
      "ds_read_b64_tr_b16 a[188:189], v6 offset:1024\n"
      "v_exp_f32_e32 v29, v29\n"
      "v_mul_f32_e32 v77, v13, v77\n"
      "v_mul_f32_e32 v78, v13, v78\n"
      "v_mul_f32_e32 v79, v13, v79\n"
      "v_mfma_f32_16x16x32_bf16 v[32:35], a[104:107], a[56:59], v[32:35]\n"
      "v_add_f32_e32 v15, v29, v15\n"
      "v_cvt_pk_bf16_f32 v42, v28, v29\n"
      "v_cvt_pk_bf16_f32 v76, v76, v77\n"
      "v_cvt_pk_bf16_f32 v77, v78, v79\n"
      "ds_write_b64 v89, v[76:77] offset:160\n"
      "v_mfma_f32_16x16x32_bf16 v[32:35], a[108:111], a[60:63], v[32:35]\n"
      "ds_read_b64_tr_b16 a[190:191], v6 offset:5120\n"
      "v_exp_f32_e32 v30, v30\n"
      "v_accvgpr_read_b32 v80, a248\n"
      "v_accvgpr_read_b32 v81, a249\n"
      "v_accvgpr_read_b32 v82, a250\n"
      "v_mfma_f32_16x16x32_bf16 v[36:39], a[112:115], a[48:51], v[60:63]\n"
      "ds_read_b128 a[128:131], v1 offset:8192\n"
      "ds_read_b128 a[132:135], v2 offset:8192\n"
      "v_add_f32_e32 v15, v30, v15\n"
      "v_accvgpr_read_b32 v83, a251\n"
      "v_mul_f32_e32 v80, v13, v80\n"
      "v_mul_f32_e32 v81, v13, v81\n"
      "v_mfma_f32_16x16x32_bf16 v[36:39], a[116:119], a[52:55], v[36:39]\n"
      "v_exp_f32_e32 v31, v31\n"
      "v_mul_f32_e32 v82, v13, v82\n"
      "v_mul_f32_e32 v83, v13, v83\n"
      "v_cvt_pk_bf16_f32 v80, v80, v81\n"
      "v_mfma_f32_16x16x32_bf16 v[36:39], a[120:123], a[56:59], v[36:39]\n"
      "v_add_f32_e32 v15, v31, v15\n"
      "v_cvt_pk_bf16_f32 v43, v30, v31\n"
      "v_cvt_pk_bf16_f32 v81, v82, v83\n"
      "ds_write_b64 v89, v[80:81] offset:192\n"
      "v_accvgpr_read_b32 v84, a252\n"
      "v_mfma_f32_16x16x32_bf16 v[36:39], a[124:127], a[60:63], v[36:39]\n"
      "v_exp_f32_e32 v32, v32\n"
      "v_accvgpr_read_b32 v85, a253\n"
      "v_accvgpr_read_b32 v86, a254\n"
      "v_accvgpr_read_b32 v87, a255\n"
      "s_waitcnt lgkmcnt(3)\n"
      "v_mfma_f32_16x16x32_bf16 v[128:131], a[160:163], v[40:43], v[128:131]\n"
      "ds_read_b128 a[136:139], v1 offset:9216\n"
      "ds_read_b128 a[140:143], v2 offset:9216\n"
      "v_add_f32_e32 v16, v32, v16\n"
      "v_mul_f32_e32 v84, v13, v84\n"
      "v_mul_f32_e32 v85, v13, v85\n"
      "v_mul_f32_e32 v86, v13, v86\n"
      "v_mfma_f32_16x16x32_bf16 v[132:135], a[164:167], v[40:43], v[132:135]\n"
      "v_exp_f32_e32 v33, v33\n"
      "v_mul_f32_e32 v87, v13, v87\n"
      "v_cvt_pk_bf16_f32 v84, v84, v85\n"
      "v_cvt_pk_bf16_f32 v85, v86, v87\n"
      "v_mfma_f32_16x16x32_bf16 v[136:139], a[168:171], v[40:43], v[136:139]\n"
      "v_add_f32_e32 v16, v33, v16\n"
      "v_cvt_pk_bf16_f32 v44, v32, v33\n"
      "ds_write_b64 v89, v[84:85] offset:224\n"
      "ds_read_b128 v[72:75], v90 offset:0\n"
      "ds_read_b128 v[76:79], v90 offset:1088\n"
      "ds_read_b128 v[80:83], v90 offset:2176\n"
      "ds_read_b128 v[84:87], v90 offset:3264\n"
      "v_mfma_f32_16x16x32_bf16 v[140:143], a[172:175], v[40:43], v[140:143]\n"
      "ds_read_b128 a[144:147], v1 offset:12288\n"
      "v_exp_f32_e32 v34, v34\n"
      "v_mov_b32_e32 v23, v14\n"
      "s_nop 1\n"
      "v_permlane16_swap_b32_e32 v23, v14\n"
      "v_mfma_f32_16x16x32_bf16 v[144:147], a[176:179], v[40:43], v[144:147]\n"
      "ds_read_b128 a[148:151], v2 offset:12288\n"
      "ds_read_b128 a[152:155], v1 offset:13312\n"
      "v_add_f32_e32 v16, v34, v16\n"
      "v_add_f32_e32 v14, v23, v14\n"
      "v_mov_b32_e32 v23, v14\n"
      "v_mfma_f32_16x16x32_bf16 v[148:151], a[180:183], v[40:43], v[148:151]\n"
      "ds_read_b128 a[156:159], v2 offset:13312\n"
      "v_exp_f32_e32 v35, v35\n"
      "v_permlane32_swap_b32_e32 v23, v14\n"
      "v_add_f32_e32 v14, v23, v14\n"
      "v_mfma_f32_16x16x32_bf16 v[152:155], a[184:187], v[40:43], v[152:155]\n"
      "v_add_f32_e32 v16, v35, v16\n"
      "v_cvt_pk_bf16_f32 v45, v34, v35\n"
      "s_mov_b32 s39, 0\n"
      "s_waitcnt lgkmcnt(7)\n"
      "buffer_store_dwordx4 v[72:75], v88, s[28:31], s39 offen sc1 nt\n"
      "v_cmp_nge_f32_e32 vcc, s46, v14\n"
      "v_rcp_f32_e32 v14, v14\n"
      "v_mfma_f32_16x16x32_bf16 v[156:159], a[188:191], v[40:43], v[156:159]\n"
      "v_exp_f32_e32 v36, v36\n"
      "s_or_b64 s[44:45], s[44:45], vcc\n"
      "v_mfma_f32_16x16x32_bf16 v[24:27], a[128:131], a[32:35], v[56:59]\n"
      "ds_read_b64_tr_b16 a[192:193], v3 offset:8192\n"
      "ds_read_b64_tr_b16 a[194:195], v3 offset:12288\n"
      "v_add_f32_e32 v16, v36, v16\n"
      "v_mul_f32_e32 v96, v14, v96\n"
      "v_mul_f32_e32 v97, v14, v97\n"
      "v_mul_f32_e32 v98, v14, v98\n"
      "v_mfma_f32_16x16x32_bf16 v[24:27], a[132:135], a[36:39], v[24:27]\n"
      "v_exp_f32_e32 v37, v37\n"
      "v_mul_f32_e32 v99, v14, v99\n"
      "v_cvt_pk_bf16_f32 v96, v96, v97\n"
      "v_cvt_pk_bf16_f32 v97, v98, v99\n"
      "v_mfma_f32_16x16x32_bf16 v[24:27], a[136:139], a[40:43], v[24:27]\n"
      "v_add_f32_e32 v16, v37, v16\n"
      "v_cvt_pk_bf16_f32 v46, v36, v37\n"
      "ds_write_b64 v89, v[96:97] offset:0\n"
      "v_mul_f32_e32 v100, v14, v100\n"
      "v_mul_f32_e32 v101, v14, v101\n"
      "v_mfma_f32_16x16x32_bf16 v[24:27], a[140:143], a[44:47], v[24:27]\n"
      "v_exp_f32_e32 v38, v38\n"
      "v_mul_f32_e32 v102, v14, v102\n"
      "v_mul_f32_e32 v103, v14, v103\n"
      "v_cvt_pk_bf16_f32 v100, v100, v101\n"
      "s_waitcnt lgkmcnt(3)\n"
      "v_mfma_f32_16x16x32_bf16 v[28:31], a[144:147], a[32:35], v[56:59]\n"
      "ds_read_b64_tr_b16 a[196:197], v4 offset:8192\n"
      "ds_read_b64_tr_b16 a[198:199], v4 offset:12288\n"
      "v_add_f32_e32 v16, v38, v16\n"
      "v_cvt_pk_bf16_f32 v101, v102, v103\n"
      "ds_write_b64 v89, v[100:101] offset:32\n"
      "v_mul_f32_e32 v104, v14, v104\n"
      "v_mfma_f32_16x16x32_bf16 v[28:31], a[148:151], a[36:39], v[28:31]\n"
      "v_exp_f32_e32 v39, v39\n"
      "v_mul_f32_e32 v105, v14, v105\n"
      "v_mul_f32_e32 v106, v14, v106\n"
      "v_mul_f32_e32 v107, v14, v107\n"
      "v_mfma_f32_16x16x32_bf16 v[28:31], a[152:155], a[40:43], v[28:31]\n"
      "v_add_f32_e32 v16, v39, v16\n"
      "v_cvt_pk_bf16_f32 v47, v38, v39\n"
      "v_cvt_pk_bf16_f32 v104, v104, v105\n"
      "v_cvt_pk_bf16_f32 v105, v106, v107\n"
      "ds_write_b64 v89, v[104:105] offset:64\n"
      "v_mfma_f32_16x16x32_bf16 v[28:31], a[156:159], a[44:47], v[28:31]\n"
      "v_exp_f32_e32 v24, v24\n"
      "v_mul_f32_e32 v108, v14, v108\n"
      "v_mul_f32_e32 v109, v14, v109\n"
      "v_mul_f32_e32 v110, v14, v110\n"
      "v_mfma_f32_16x16x32_bf16 v[160:163], a[160:163], v[44:47], v[160:163]\n"
      "ds_read_b64_tr_b16 a[200:201], v5 offset:8192\n"
      "ds_read_b64_tr_b16 a[202:203], v5 offset:12288\n"
      "v_add_f32_e32 v15, v24, v15\n"
      "buffer_store_dwordx4 v[76:79], v88, s[28:31], s39 offen offset:1024 sc1 nt\n"
      "v_mul_f32_e32 v111, v14, v111\n"
      "v_cvt_pk_bf16_f32 v108, v108, v109\n"
      "v_cvt_pk_bf16_f32 v109, v110, v111\n"
      "v_mfma_f32_16x16x32_bf16 v[164:167], a[164:167], v[44:47], v[164:167]\n"
      "ds_read_b64_tr_b16 a[204:205], v6 offset:8192\n"
      "v_exp_f32_e32 v25, v25\n"
      "ds_write_b64 v89, v[108:109] offset:96\n"
      "v_mul_f32_e32 v112, v14, v112\n"
      "v_mul_f32_e32 v113, v14, v113\n"
      "v_mfma_f32_16x16x32_bf16 v[168:171], a[168:171], v[44:47], v[168:171]\n"
      "v_add_f32_e32 v15, v25, v15\n"
      "v_cvt_pk_bf16_f32 v40, v24, v25\n"
      "v_mul_f32_e32 v114, v14, v114\n"
      "v_mul_f32_e32 v115, v14, v115\n"
      "v_cvt_pk_bf16_f32 v112, v112, v113\n"
      "v_mfma_f32_16x16x32_bf16 v[172:175], a[172:175], v[44:47], v[172:175]\n"
      "ds_read_b64_tr_b16 a[206:207], v6 offset:12288\n"
      "v_exp_f32_e32 v26, v26\n"
      "v_cvt_pk_bf16_f32 v113, v114, v115\n"
      "ds_write_b64 v89, v[112:113] offset:128\n"
      "v_mul_f32_e32 v116, v14, v116\n"
      "v_mfma_f32_16x16x32_bf16 v[176:179], a[176:179], v[44:47], v[176:179]\n"
      "ds_read_b64_tr_b16 a[208:209], v3 offset:9216\n"
      "ds_read_b64_tr_b16 a[210:211], v3 offset:13312\n"
      "v_add_f32_e32 v15, v26, v15\n"
      "v_mul_f32_e32 v117, v14, v117\n"
      "v_mul_f32_e32 v118, v14, v118\n"
      "v_mul_f32_e32 v119, v14, v119\n"
      "v_mfma_f32_16x16x32_bf16 v[180:183], a[180:183], v[44:47], v[180:183]\n"
      "ds_read_b64_tr_b16 a[212:213], v4 offset:9216\n"
      "v_exp_f32_e32 v27, v27\n"
      "v_cvt_pk_bf16_f32 v116, v116, v117\n"
      "v_cvt_pk_bf16_f32 v117, v118, v119\n"
      "ds_write_b64 v89, v[116:117] offset:160\n"
      "v_mfma_f32_16x16x32_bf16 v[184:187], a[184:187], v[44:47], v[184:187]\n"
      "v_add_f32_e32 v15, v27, v15\n"
      "v_cvt_pk_bf16_f32 v41, v26, v27\n"
      "v_mul_f32_e32 v120, v14, v120\n"
      "v_mul_f32_e32 v121, v14, v121\n"
      "v_mul_f32_e32 v122, v14, v122\n"
      "v_mfma_f32_16x16x32_bf16 v[188:191], a[188:191], v[44:47], v[188:191]\n"
      "ds_read_b64_tr_b16 a[214:215], v4 offset:13312\n"
      "v_exp_f32_e32 v28, v28\n"
      "v_mul_f32_e32 v123, v14, v123\n"
      "v_cvt_pk_bf16_f32 v120, v120, v121\n"
      "v_cvt_pk_bf16_f32 v121, v122, v123\n"
      "v_mfma_f32_16x16x32_bf16 v[32:35], a[128:131], a[48:51], v[60:63]\n"
      "ds_read_b64_tr_b16 a[216:217], v5 offset:9216\n"
      "ds_read_b64_tr_b16 a[218:219], v5 offset:13312\n"
      "v_add_f32_e32 v15, v28, v15\n"
      "ds_write_b64 v89, v[120:121] offset:192\n"
      "v_mul_f32_e32 v124, v14, v124\n"
      "v_mul_f32_e32 v125, v14, v125\n"
      "v_mfma_f32_16x16x32_bf16 v[32:35], a[132:135], a[52:55], v[32:35]\n"
      "ds_read_b64_tr_b16 a[220:221], v6 offset:9216\n"
      "v_exp_f32_e32 v29, v29\n"
      "v_mul_f32_e32 v126, v14, v126\n"
      "v_mul_f32_e32 v127, v14, v127\n"
      "v_cvt_pk_bf16_f32 v124, v124, v125\n"
      "v_mfma_f32_16x16x32_bf16 v[32:35], a[136:139], a[56:59], v[32:35]\n"
      "v_add_f32_e32 v15, v29, v15\n"
      "v_cvt_pk_bf16_f32 v42, v28, v29\n"
      "buffer_store_dwordx4 v[80:83], v88, s[28:31], s39 offen offset:2048 sc1 nt\n"
      "v_cvt_pk_bf16_f32 v125, v126, v127\n"
      "ds_write_b64 v89, v[124:125] offset:224\n"
      "ds_read_b128 v[112:115], v90 offset:0\n"
      "ds_read_b128 v[116:119], v90 offset:1088\n"
      "ds_read_b128 v[120:123], v90 offset:2176\n"
      "ds_read_b128 v[124:127], v90 offset:3264\n"
      "v_mfma_f32_16x16x32_bf16 v[32:35], a[140:143], a[60:63], v[32:35]\n"
      "ds_read_b64_tr_b16 a[222:223], v6 offset:13312\n"
      "v_exp_f32_e32 v30, v30\n"
      "v_mfma_f32_16x16x32_bf16 v[36:39], a[144:147], a[48:51], v[60:63]\n"
      "ds_read_b128 a[96:99], v1 offset:16384\n"
      "ds_read_b128 a[100:103], v2 offset:16384\n"
      "v_add_f32_e32 v15, v30, v15\n"
      "v_mfma_f32_16x16x32_bf16 v[36:39], a[148:151], a[52:55], v[36:39]\n"
      "v_exp_f32_e32 v31, v31\n"
      "v_mfma_f32_16x16x32_bf16 v[36:39], a[152:155], a[56:59], v[36:39]\n"
      "v_add_f32_e32 v15, v31, v15\n"
      "v_cvt_pk_bf16_f32 v43, v30, v31\n"
      "v_mfma_f32_16x16x32_bf16 v[36:39], a[156:159], a[60:63], v[36:39]\n"
      "v_exp_f32_e32 v32, v32\n"
      "s_waitcnt lgkmcnt(2)\n"
      "v_mfma_f32_16x16x32_bf16 v[128:131], a[192:195], v[40:43], v[128:131]\n"
      "ds_read_b128 a[104:107], v1 offset:17408\n"
      "ds_read_b128 a[108:111], v2 offset:17408\n"
      "v_add_f32_e32 v16, v32, v16\n"
      "v_mfma_f32_16x16x32_bf16 v[132:135], a[196:199], v[40:43], v[132:135]\n"
      "v_exp_f32_e32 v33, v33\n"
      "v_mfma_f32_16x16x32_bf16 v[136:139], a[200:203], v[40:43], v[136:139]\n"
      "v_add_f32_e32 v16, v33, v16\n"
      "v_cvt_pk_bf16_f32 v44, v32, v33\n"
      "v_mfma_f32_16x16x32_bf16 v[140:143], a[204:207], v[40:43], v[140:143]\n"
      "ds_read_b128 a[112:115], v1 offset:20480\n"
      "v_exp_f32_e32 v34, v34\n"
      "v_mfma_f32_16x16x32_bf16 v[144:147], a[208:211], v[40:43], v[144:147]\n"
      "ds_read_b128 a[116:119], v2 offset:20480\n"
      "ds_read_b128 a[120:123], v1 offset:21504\n"
      "v_add_f32_e32 v16, v34, v16\n"
      "buffer_store_dwordx4 v[84:87], v88, s[28:31], s39 offen offset:3072 sc1 nt\n"
      "v_mfma_f32_16x16x32_bf16 v[148:151], a[212:215], v[40:43], v[148:151]\n"
      "ds_read_b128 a[124:127], v2 offset:21504\n"
      "v_exp_f32_e32 v35, v35\n"
      "v_mfma_f32_16x16x32_bf16 v[152:155], a[216:219], v[40:43], v[152:155]\n"
      "v_add_f32_e32 v16, v35, v16\n"
      "v_cvt_pk_bf16_f32 v45, v34, v35\n"
      "v_mfma_f32_16x16x32_bf16 v[156:159], a[220:223], v[40:43], v[156:159]\n"
      "v_exp_f32_e32 v36, v36\n"
      "s_waitcnt lgkmcnt(3)\n"
      "v_mfma_f32_16x16x32_bf16 v[24:27], a[96:99], a[32:35], v[56:59]\n"
      "ds_read_b64_tr_b16 a[160:161], v3 offset:16384\n"
      "ds_read_b64_tr_b16 a[162:163], v3 offset:20480\n"
      "v_add_f32_e32 v16, v36, v16\n"
      "v_mfma_f32_16x16x32_bf16 v[24:27], a[100:103], a[36:39], v[24:27]\n"
      "v_exp_f32_e32 v37, v37\n"
      "v_mfma_f32_16x16x32_bf16 v[24:27], a[104:107], a[40:43], v[24:27]\n"
      "v_add_f32_e32 v16, v37, v16\n"
      "v_cvt_pk_bf16_f32 v46, v36, v37\n"
      "v_mfma_f32_16x16x32_bf16 v[24:27], a[108:111], a[44:47], v[24:27]\n"
      "v_exp_f32_e32 v38, v38\n"
      "v_mfma_f32_16x16x32_bf16 v[28:31], a[112:115], a[32:35], v[56:59]\n"
      "ds_read_b64_tr_b16 a[164:165], v4 offset:16384\n"
      "ds_read_b64_tr_b16 a[166:167], v4 offset:20480\n"
      "v_add_f32_e32 v16, v38, v16\n"
      "s_waitcnt lgkmcnt(2)\n"
      "v_mfma_f32_16x16x32_bf16 v[28:31], a[116:119], a[36:39], v[28:31]\n"
      "v_exp_f32_e32 v39, v39\n"
      "v_mfma_f32_16x16x32_bf16 v[28:31], a[120:123], a[40:43], v[28:31]\n"
      "v_add_f32_e32 v16, v39, v16\n"
      "v_cvt_pk_bf16_f32 v47, v38, v39\n"
      "s_mov_b32 s39, 4096\n"
      "buffer_store_dwordx4 v[112:115], v88, s[28:31], s39 offen sc1 nt\n"
      "v_mfma_f32_16x16x32_bf16 v[28:31], a[124:127], a[44:47], v[28:31]\n"
      "v_exp_f32_e32 v24, v24\n"
      "v_mfma_f32_16x16x32_bf16 v[160:163], a[192:195], v[44:47], v[160:163]\n"
      "ds_read_b64_tr_b16 a[168:169], v5 offset:16384\n"
      "ds_read_b64_tr_b16 a[170:171], v5 offset:20480\n"
      "v_add_f32_e32 v15, v24, v15\n"
      "v_mfma_f32_16x16x32_bf16 v[164:167], a[196:199], v[44:47], v[164:167]\n"
      "ds_read_b64_tr_b16 a[172:173], v6 offset:16384\n"
      "v_exp_f32_e32 v25, v25\n"
      "v_mfma_f32_16x16x32_bf16 v[168:171], a[200:203], v[44:47], v[168:171]\n"
      "v_add_f32_e32 v15, v25, v15\n"
      "v_cvt_pk_bf16_f32 v40, v24, v25\n"
      "v_mfma_f32_16x16x32_bf16 v[172:175], a[204:207], v[44:47], v[172:175]\n"
      "ds_read_b64_tr_b16 a[174:175], v6 offset:20480\n"
      "v_exp_f32_e32 v26, v26\n"
      "v_mfma_f32_16x16x32_bf16 v[176:179], a[208:211], v[44:47], v[176:179]\n"
      "ds_read_b64_tr_b16 a[176:177], v3 offset:17408\n"
      "ds_read_b64_tr_b16 a[178:179], v3 offset:21504\n"
      "v_add_f32_e32 v15, v26, v15\n"
      "v_mfma_f32_16x16x32_bf16 v[180:183], a[212:215], v[44:47], v[180:183]\n"
      "ds_read_b64_tr_b16 a[180:181], v4 offset:17408\n"
      "v_exp_f32_e32 v27, v27\n"
      "v_mfma_f32_16x16x32_bf16 v[184:187], a[216:219], v[44:47], v[184:187]\n"
      "v_add_f32_e32 v15, v27, v15\n"
      "v_cvt_pk_bf16_f32 v41, v26, v27\n"
      "v_mfma_f32_16x16x32_bf16 v[188:191], a[220:223], v[44:47], v[188:191]\n"
      "ds_read_b64_tr_b16 a[182:183], v4 offset:21504\n"
      "v_exp_f32_e32 v28, v28\n"
      "v_mfma_f32_16x16x32_bf16 v[32:35], a[96:99], a[48:51], v[60:63]\n"
      "ds_read_b64_tr_b16 a[184:185], v5 offset:17408\n"
      "ds_read_b64_tr_b16 a[186:187], v5 offset:21504\n"
      "v_add_f32_e32 v15, v28, v15\n"
      "buffer_store_dwordx4 v[116:119], v88, s[28:31], s39 offen offset:1024 sc1 nt\n"
      "v_mfma_f32_16x16x32_bf16 v[32:35], a[100:103], a[52:55], v[32:35]\n"
      "ds_read_b64_tr_b16 a[188:189], v6 offset:17408\n"
      "v_exp_f32_e32 v29, v29\n"
      "v_mfma_f32_16x16x32_bf16 v[32:35], a[104:107], a[56:59], v[32:35]\n"
      "v_add_f32_e32 v15, v29, v15\n"
      "v_cvt_pk_bf16_f32 v42, v28, v29\n"
      "v_mfma_f32_16x16x32_bf16 v[32:35], a[108:111], a[60:63], v[32:35]\n"
      "ds_read_b64_tr_b16 a[190:191], v6 offset:21504\n"
      "v_exp_f32_e32 v30, v30\n"
      "v_mfma_f32_16x16x32_bf16 v[36:39], a[112:115], a[48:51], v[60:63]\n"
      "ds_read_b128 a[128:131], v1 offset:24576\n"
      "ds_read_b128 a[132:135], v2 offset:24576\n"
      "v_add_f32_e32 v15, v30, v15\n"
      "v_mfma_f32_16x16x32_bf16 v[36:39], a[116:119], a[52:55], v[36:39]\n"
      "v_exp_f32_e32 v31, v31\n"
      "v_mfma_f32_16x16x32_bf16 v[36:39], a[120:123], a[56:59], v[36:39]\n"
      "v_add_f32_e32 v15, v31, v15\n"
      "v_cvt_pk_bf16_f32 v43, v30, v31\n"
      "v_mfma_f32_16x16x32_bf16 v[36:39], a[124:127], a[60:63], v[36:39]\n"
      "v_exp_f32_e32 v32, v32\n"
      "v_mfma_f32_16x16x32_bf16 v[128:131], a[160:163], v[40:43], v[128:131]\n"
      "ds_read_b128 a[136:139], v1 offset:25600\n"
      "ds_read_b128 a[140:143], v2 offset:25600\n"
      "v_add_f32_e32 v16, v32, v16\n"
      "s_waitcnt lgkmcnt(2)\n"
      "v_mfma_f32_16x16x32_bf16 v[132:135], a[164:167], v[40:43], v[132:135]\n"
      "v_exp_f32_e32 v33, v33\n"
      "v_mfma_f32_16x16x32_bf16 v[136:139], a[168:171], v[40:43], v[136:139]\n"
      "v_add_f32_e32 v16, v33, v16\n"
      "v_cvt_pk_bf16_f32 v44, v32, v33\n"
      "buffer_store_dwordx4 v[120:123], v88, s[28:31], s39 offen offset:2048 sc1 nt\n"
      "v_mfma_f32_16x16x32_bf16 v[140:143], a[172:175], v[40:43], v[140:143]\n"
      "ds_read_b128 a[144:147], v1 offset:28672\n"
      "v_exp_f32_e32 v34, v34\n"
      "v_mfma_f32_16x16x32_bf16 v[144:147], a[176:179], v[40:43], v[144:147]\n"
      "ds_read_b128 a[148:151], v2 offset:28672\n"
      "ds_read_b128 a[152:155], v1 offset:29696\n"
      "v_add_f32_e32 v16, v34, v16\n"
      "v_mfma_f32_16x16x32_bf16 v[148:151], a[180:183], v[40:43], v[148:151]\n"
      "ds_read_b128 a[156:159], v2 offset:29696\n"
      "v_exp_f32_e32 v35, v35\n"
      "v_mfma_f32_16x16x32_bf16 v[152:155], a[184:187], v[40:43], v[152:155]\n"
      "v_add_f32_e32 v16, v35, v16\n"
      "v_cvt_pk_bf16_f32 v45, v34, v35\n"
      "v_mfma_f32_16x16x32_bf16 v[156:159], a[188:191], v[40:43], v[156:159]\n"
      "v_exp_f32_e32 v36, v36\n"
      "v_mfma_f32_16x16x32_bf16 v[24:27], a[128:131], a[32:35], v[56:59]\n"
      "ds_read_b64_tr_b16 a[192:193], v3 offset:24576\n"
      "ds_read_b64_tr_b16 a[194:195], v3 offset:28672\n"
      "v_add_f32_e32 v16, v36, v16\n"
      "v_mfma_f32_16x16x32_bf16 v[24:27], a[132:135], a[36:39], v[24:27]\n"
      "v_exp_f32_e32 v37, v37\n"
      "s_waitcnt lgkmcnt(2)\n"
      "v_mfma_f32_16x16x32_bf16 v[24:27], a[136:139], a[40:43], v[24:27]\n"
      "v_add_f32_e32 v16, v37, v16\n"
      "v_cvt_pk_bf16_f32 v46, v36, v37\n"
      "v_mfma_f32_16x16x32_bf16 v[24:27], a[140:143], a[44:47], v[24:27]\n"
      "v_exp_f32_e32 v38, v38\n"
      "v_mfma_f32_16x16x32_bf16 v[28:31], a[144:147], a[32:35], v[56:59]\n"
      "ds_read_b64_tr_b16 a[196:197], v4 offset:24576\n"
      "ds_read_b64_tr_b16 a[198:199], v4 offset:28672\n"
      "v_add_f32_e32 v16, v38, v16\n"
      "buffer_store_dwordx4 v[124:127], v88, s[28:31], s39 offen offset:3072 sc1 nt\n"
      "v_mfma_f32_16x16x32_bf16 v[28:31], a[148:151], a[36:39], v[28:31]\n"
      "v_exp_f32_e32 v39, v39\n"
      "v_mfma_f32_16x16x32_bf16 v[28:31], a[152:155], a[40:43], v[28:31]\n"
      "v_add_f32_e32 v16, v39, v16\n"
      "v_cvt_pk_bf16_f32 v47, v38, v39\n"
      "v_mfma_f32_16x16x32_bf16 v[28:31], a[156:159], a[44:47], v[28:31]\n"
      "v_exp_f32_e32 v24, v24\n"
      "v_mfma_f32_16x16x32_bf16 v[160:163], a[160:163], v[44:47], v[160:163]\n"
      "ds_read_b64_tr_b16 a[200:201], v5 offset:24576\n"
      "ds_read_b64_tr_b16 a[202:203], v5 offset:28672\n"
      "v_add_f32_e32 v15, v24, v15\n"
      "v_mfma_f32_16x16x32_bf16 v[164:167], a[164:167], v[44:47], v[164:167]\n"
      "ds_read_b64_tr_b16 a[204:205], v6 offset:24576\n"
      "v_exp_f32_e32 v25, v25\n"
      "v_mfma_f32_16x16x32_bf16 v[168:171], a[168:171], v[44:47], v[168:171]\n"
      "v_add_f32_e32 v15, v25, v15\n"
      "v_cvt_pk_bf16_f32 v40, v24, v25\n"
      "v_mfma_f32_16x16x32_bf16 v[172:175], a[172:175], v[44:47], v[172:175]\n"
      "ds_read_b64_tr_b16 a[206:207], v6 offset:28672\n"
      "v_exp_f32_e32 v26, v26\n"
      "v_mfma_f32_16x16x32_bf16 v[176:179], a[176:179], v[44:47], v[176:179]\n"
      "ds_read_b64_tr_b16 a[208:209], v3 offset:25600\n"
      "ds_read_b64_tr_b16 a[210:211], v3 offset:29696\n"
      "v_add_f32_e32 v15, v26, v15\n"
      "v_mfma_f32_16x16x32_bf16 v[180:183], a[180:183], v[44:47], v[180:183]\n"
      "ds_read_b64_tr_b16 a[212:213], v4 offset:25600\n"
      "v_exp_f32_e32 v27, v27\n"
      "v_mfma_f32_16x16x32_bf16 v[184:187], a[184:187], v[44:47], v[184:187]\n"
      "v_add_f32_e32 v15, v27, v15\n"
      "v_cvt_pk_bf16_f32 v41, v26, v27\n"
      "v_mfma_f32_16x16x32_bf16 v[188:191], a[188:191], v[44:47], v[188:191]\n"
      "ds_read_b64_tr_b16 a[214:215], v4 offset:29696\n"
      "v_exp_f32_e32 v28, v28\n"
      "v_mfma_f32_16x16x32_bf16 v[32:35], a[128:131], a[48:51], v[60:63]\n"
      "ds_read_b64_tr_b16 a[216:217], v5 offset:25600\n"
      "ds_read_b64_tr_b16 a[218:219], v5 offset:29696\n"
      "v_add_f32_e32 v15, v28, v15\n"
      "v_mfma_f32_16x16x32_bf16 v[32:35], a[132:135], a[52:55], v[32:35]\n"
      "ds_read_b64_tr_b16 a[220:221], v6 offset:25600\n"
      "v_exp_f32_e32 v29, v29\n"
      "v_mfma_f32_16x16x32_bf16 v[32:35], a[136:139], a[56:59], v[32:35]\n"
      "v_add_f32_e32 v15, v29, v15\n"
      "v_cvt_pk_bf16_f32 v42, v28, v29\n"
      "v_mfma_f32_16x16x32_bf16 v[32:35], a[140:143], a[60:63], v[32:35]\n"
      "ds_read_b64_tr_b16 a[222:223], v6 offset:29696\n"
      "v_exp_f32_e32 v30, v30\n"
      "v_mfma_f32_16x16x32_bf16 v[36:39], a[144:147], a[48:51], v[60:63]\n"
      "ds_read_b128 a[96:99], v1 offset:0\n"
      "ds_read_b128 a[100:103], v2 offset:0\n"
      "v_add_f32_e32 v15, v30, v15\n"
      "v_mfma_f32_16x16x32_bf16 v[36:39], a[148:151], a[52:55], v[36:39]\n"
      "v_exp_f32_e32 v31, v31\n"
      "v_mfma_f32_16x16x32_bf16 v[36:39], a[152:155], a[56:59], v[36:39]\n"
      "v_add_f32_e32 v15, v31, v15\n"
      "v_cvt_pk_bf16_f32 v43, v30, v31\n"
      "v_mfma_f32_16x16x32_bf16 v[36:39], a[156:159], a[60:63], v[36:39]\n"
      "v_exp_f32_e32 v32, v32\n"
      "s_waitcnt lgkmcnt(2)\n"
      "v_mfma_f32_16x16x32_bf16 v[128:131], a[192:195], v[40:43], v[128:131]\n"
      "ds_read_b128 a[104:107], v1 offset:1024\n"
      "ds_read_b128 a[108:111], v2 offset:1024\n"
      "v_add_f32_e32 v16, v32, v16\n"
      "v_mov_b32_e32 v23, v15\n"
      "s_nop 1\n"
      "v_permlane16_swap_b32_e32 v23, v15\n"
      "v_mfma_f32_16x16x32_bf16 v[132:135], a[196:199], v[40:43], v[132:135]\n"
      "v_exp_f32_e32 v33, v33\n"
      "v_add_f32_e32 v15, v23, v15\n"
      "v_mov_b32_e32 v23, v15\n"
      "v_mfma_f32_16x16x32_bf16 v[136:139], a[200:203], v[40:43], v[136:139]\n"
      "v_add_f32_e32 v16, v33, v16\n"
      "v_cvt_pk_bf16_f32 v44, v32, v33\n"
      "v_permlane32_swap_b32_e32 v23, v15\n"
      "v_add_f32_e32 v15, v23, v15\n"
      "v_mfma_f32_16x16x32_bf16 v[140:143], a[204:207], v[40:43], v[140:143]\n"
      "ds_read_b128 a[112:115], v1 offset:4096\n"
      "v_exp_f32_e32 v34, v34\n"
      "v_cmp_nge_f32_e32 vcc, s46, v15\n"
      "v_rcp_f32_e32 v15, v15\n"
      "v_mfma_f32_16x16x32_bf16 v[144:147], a[208:211], v[40:43], v[144:147]\n"
      "ds_read_b128 a[116:119], v2 offset:4096\n"
      "ds_read_b128 a[120:123], v1 offset:5120\n"
      "v_add_f32_e32 v16, v34, v16\n"
      "s_or_b64 s[44:45], s[44:45], vcc\n"
      "v_mfma_f32_16x16x32_bf16 v[148:151], a[212:215], v[40:43], v[148:151]\n"
      "ds_read_b128 a[124:127], v2 offset:5120\n"
      "v_exp_f32_e32 v35, v35\n"
      "v_mul_f32_e32 v128, v15, v128\n"
      "v_mul_f32_e32 v129, v15, v129\n"
      "v_mul_f32_e32 v130, v15, v130\n"
      "v_mfma_f32_16x16x32_bf16 v[152:155], a[216:219], v[40:43], v[152:155]\n"
      "v_add_f32_e32 v16, v35, v16\n"
      "v_cvt_pk_bf16_f32 v45, v34, v35\n"
      "v_mul_f32_e32 v131, v15, v131\n"
      "v_cvt_pk_bf16_f32 v128, v128, v129\n"
      "v_cvt_pk_bf16_f32 v129, v130, v131\n"
      "v_mfma_f32_16x16x32_bf16 v[156:159], a[220:223], v[40:43], v[156:159]\n"
      "v_exp_f32_e32 v36, v36\n"
      "ds_write_b64 v89, v[128:129] offset:0\n"
      "v_mul_f32_e32 v132, v15, v132\n"
      "v_mul_f32_e32 v133, v15, v133\n"
      "s_waitcnt lgkmcnt(4)\n"
      "v_mfma_f32_16x16x32_bf16 v[24:27], a[96:99], a[64:67], v[64:67]\n"
      "ds_read_b64_tr_b16 a[160:161], v3 offset:0\n"
      "ds_read_b64_tr_b16 a[162:163], v3 offset:4096\n"
      "v_add_f32_e32 v16, v36, v16\n"
      "v_mul_f32_e32 v134, v15, v134\n"
      "v_mul_f32_e32 v135, v15, v135\n"
      "v_cvt_pk_bf16_f32 v132, v132, v133\n"
      "v_mfma_f32_16x16x32_bf16 v[24:27], a[100:103], a[68:71], v[24:27]\n"
      "v_exp_f32_e32 v37, v37\n"
      "v_cvt_pk_bf16_f32 v133, v134, v135\n"
      "ds_write_b64 v89, v[132:133] offset:32\n"
      "v_mul_f32_e32 v136, v15, v136\n"
      "v_mfma_f32_16x16x32_bf16 v[24:27], a[104:107], a[72:75], v[24:27]\n"
      "v_add_f32_e32 v16, v37, v16\n"
      "v_cvt_pk_bf16_f32 v46, v36, v37\n"
      "v_mul_f32_e32 v137, v15, v137\n"
      "v_mul_f32_e32 v138, v15, v138\n"
      "v_mul_f32_e32 v139, v15, v139\n"
      "v_mfma_f32_16x16x32_bf16 v[24:27], a[108:111], a[76:79], v[24:27]\n"
      "v_exp_f32_e32 v38, v38\n"
      "v_cvt_pk_bf16_f32 v136, v136, v137\n"
      "v_cvt_pk_bf16_f32 v137, v138, v139\n"
      "ds_write_b64 v89, v[136:137] offset:64\n"
      "v_mfma_f32_16x16x32_bf16 v[28:31], a[112:115], a[64:67], v[64:67]\n"
      "ds_read_b64_tr_b16 a[164:165], v4 offset:0\n"
      "ds_read_b64_tr_b16 a[166:167], v4 offset:4096\n"
      "v_add_f32_e32 v16, v38, v16\n"
      "v_mul_f32_e32 v140, v15, v140\n"
      "v_mul_f32_e32 v141, v15, v141\n"
      "v_mul_f32_e32 v142, v15, v142\n"
      "s_waitcnt lgkmcnt(4)\n"
      "v_mfma_f32_16x16x32_bf16 v[28:31], a[116:119], a[68:71], v[28:31]\n"
      "v_exp_f32_e32 v39, v39\n"
      "v_mul_f32_e32 v143, v15, v143\n"
      "v_cvt_pk_bf16_f32 v140, v140, v141\n"
      "v_cvt_pk_bf16_f32 v141, v142, v143\n"
      "v_mfma_f32_16x16x32_bf16 v[28:31], a[120:123], a[72:75], v[28:31]\n"
      "v_add_f32_e32 v16, v39, v16\n"
      "v_cvt_pk_bf16_f32 v47, v38, v39\n"
      "ds_write_b64 v89, v[140:141] offset:96\n"
      "v_mul_f32_e32 v144, v15, v144\n"
      "v_mul_f32_e32 v145, v15, v145\n"
      "v_mfma_f32_16x16x32_bf16 v[28:31], a[124:127], a[76:79], v[28:31]\n"
      "v_exp_f32_e32 v24, v24\n"
      "v_mul_f32_e32 v146, v15, v146\n"
      "v_mul_f32_e32 v147, v15, v147\n"
      "v_cvt_pk_bf16_f32 v144, v144, v145\n"
      "v_mfma_f32_16x16x32_bf16 v[160:163], a[192:195], v[44:47], v[160:163]\n"
      "ds_read_b64_tr_b16 a[168:169], v5 offset:0\n"
      "ds_read_b64_tr_b16 a[170:171], v5 offset:4096\n"
      "v_add_f32_e32 v17, v24, v17\n"
      "v_cvt_pk_bf16_f32 v145, v146, v147\n"
      "ds_write_b64 v89, v[144:145] offset:128\n"
      "v_mul_f32_e32 v148, v15, v148\n"
      "v_mfma_f32_16x16x32_bf16 v[164:167], a[196:199], v[44:47], v[164:167]\n"
      "ds_read_b64_tr_b16 a[172:173], v6 offset:0\n"
      "v_exp_f32_e32 v25, v25\n"
      "v_mul_f32_e32 v149, v15, v149\n"
      "v_mul_f32_e32 v150, v15, v150\n"
      "v_mul_f32_e32 v151, v15, v151\n"
      "v_mfma_f32_16x16x32_bf16 v[168:171], a[200:203], v[44:47], v[168:171]\n"
      "v_add_f32_e32 v17, v25, v17\n"
      "v_cvt_pk_bf16_f32 v40, v24, v25\n"
      "v_cvt_pk_bf16_f32 v148, v148, v149\n"
      "v_cvt_pk_bf16_f32 v149, v150, v151\n"
      "ds_write_b64 v89, v[148:149] offset:160\n"
      "v_mfma_f32_16x16x32_bf16 v[172:175], a[204:207], v[44:47], v[172:175]\n"
      "ds_read_b64_tr_b16 a[174:175], v6 offset:4096\n"
      "v_exp_f32_e32 v26, v26\n"
      "v_mul_f32_e32 v152, v15, v152\n"
      "v_mul_f32_e32 v153, v15, v153\n"
      "v_mul_f32_e32 v154, v15, v154\n"
      "v_mfma_f32_16x16x32_bf16 v[176:179], a[208:211], v[44:47], v[176:179]\n"
      "ds_read_b64_tr_b16 a[176:177], v3 offset:1024\n"
      "ds_read_b64_tr_b16 a[178:179], v3 offset:5120\n"
      "v_add_f32_e32 v17, v26, v17\n"
      "v_mul_f32_e32 v155, v15, v155\n"
      "v_cvt_pk_bf16_f32 v152, v152, v153\n"
      "v_cvt_pk_bf16_f32 v153, v154, v155\n"
      "v_mfma_f32_16x16x32_bf16 v[180:183], a[212:215], v[44:47], v[180:183]\n"
      "ds_read_b64_tr_b16 a[180:181], v4 offset:1024\n"
      "v_exp_f32_e32 v27, v27\n"
      "ds_write_b64 v89, v[152:153] offset:192\n"
      "v_mul_f32_e32 v156, v15, v156\n"
      "v_mul_f32_e32 v157, v15, v157\n"
      "v_mfma_f32_16x16x32_bf16 v[184:187], a[216:219], v[44:47], v[184:187]\n"
      "v_add_f32_e32 v17, v27, v17\n"
      "v_cvt_pk_bf16_f32 v41, v26, v27\n"
      "v_mul_f32_e32 v158, v15, v158\n"
      "v_mul_f32_e32 v159, v15, v159\n"
      "v_cvt_pk_bf16_f32 v156, v156, v157\n"
      "v_mfma_f32_16x16x32_bf16 v[188:191], a[220:223], v[44:47], v[188:191]\n"
      "ds_read_b64_tr_b16 a[182:183], v4 offset:5120\n"
      "v_exp_f32_e32 v28, v28\n"
      "v_cvt_pk_bf16_f32 v157, v158, v159\n"
      "ds_write_b64 v89, v[156:157] offset:224\n"
      "ds_read_b128 v[144:147], v90 offset:0\n"
      "ds_read_b128 v[148:151], v90 offset:1088\n"
      "ds_read_b128 v[152:155], v90 offset:2176\n"
      "ds_read_b128 v[156:159], v90 offset:3264\n"
      "v_mfma_f32_16x16x32_bf16 v[32:35], a[96:99], a[80:83], v[68:71]\n"
      "ds_read_b64_tr_b16 a[184:185], v5 offset:1024\n"
      "ds_read_b64_tr_b16 a[186:187], v5 offset:5120\n"
      "v_add_f32_e32 v17, v28, v17\n"
      "v_mov_b32_e32 v23, v16\n"
      "s_nop 1\n"
      "v_permlane16_swap_b32_e32 v23, v16\n"
      "v_mfma_f32_16x16x32_bf16 v[32:35], a[100:103], a[84:87], v[32:35]\n"
      "ds_read_b64_tr_b16 a[188:189], v6 offset:1024\n"
      "v_exp_f32_e32 v29, v29\n"
      "v_add_f32_e32 v16, v23, v16\n"
      "v_mov_b32_e32 v23, v16\n"
      "v_mfma_f32_16x16x32_bf16 v[32:35], a[104:107], a[88:91], v[32:35]\n"
      "v_add_f32_e32 v17, v29, v17\n"
      "v_cvt_pk_bf16_f32 v42, v28, v29\n"
      "v_permlane32_swap_b32_e32 v23, v16\n"
      "v_add_f32_e32 v16, v23, v16\n"
      "v_mfma_f32_16x16x32_bf16 v[32:35], a[108:111], a[92:95], v[32:35]\n"
      "ds_read_b64_tr_b16 a[190:191], v6 offset:5120\n"
      "v_exp_f32_e32 v30, v30\n"
      "s_mov_b32 s39, 8192\n"
      "s_waitcnt lgkmcnt(7)\n"
      "buffer_store_dwordx4 v[144:147], v88, s[28:31], s39 offen sc1 nt\n"
      "v_cmp_nge_f32_e32 vcc, s46, v16\n"
      "v_rcp_f32_e32 v16, v16\n"
      "v_mfma_f32_16x16x32_bf16 v[36:39], a[112:115], a[80:83], v[68:71]\n"
      "ds_read_b128 a[128:131], v1 offset:8192\n"
      "ds_read_b128 a[132:135], v2 offset:8192\n"
      "v_add_f32_e32 v17, v30, v17\n"
      "s_or_b64 s[44:45], s[44:45], vcc\n"
      "v_mfma_f32_16x16x32_bf16 v[36:39], a[116:119], a[84:87], v[36:39]\n"
      "v_exp_f32_e32 v31, v31\n"
      "v_mul_f32_e32 v160, v16, v160\n"
      "v_mul_f32_e32 v161, v16, v161\n"
      "v_mul_f32_e32 v162, v16, v162\n"
      "v_mfma_f32_16x16x32_bf16 v[36:39], a[120:123], a[88:91], v[36:39]\n"
      "v_add_f32_e32 v17, v31, v17\n"
      "v_cvt_pk_bf16_f32 v43, v30, v31\n"
      "v_mul_f32_e32 v163, v16, v163\n"
      "v_cvt_pk_bf16_f32 v160, v160, v161\n"
      "v_cvt_pk_bf16_f32 v161, v162, v163\n"
      "v_mfma_f32_16x16x32_bf16 v[36:39], a[124:127], a[92:95], v[36:39]\n"
      "v_exp_f32_e32 v32, v32\n"
      "ds_write_b64 v89, v[160:161] offset:0\n"
      "v_mul_f32_e32 v164, v16, v164\n"
      "v_mul_f32_e32 v165, v16, v165\n"
      "v_mfma_f32_16x16x32_bf16 v[192:195], a[160:163], v[40:43], v[192:195]\n"
      "ds_read_b128 a[136:139], v1 offset:9216\n"
      "ds_read_b128 a[140:143], v2 offset:9216\n"
      "v_add_f32_e32 v18, v32, v18\n"
      "v_mul_f32_e32 v166, v16, v166\n"
      "v_mul_f32_e32 v167, v16, v167\n"
      "v_cvt_pk_bf16_f32 v164, v164, v165\n"
      "v_mfma_f32_16x16x32_bf16 v[196:199], a[164:167], v[40:43], v[196:199]\n"
      "v_exp_f32_e32 v33, v33\n"
      "v_cvt_pk_bf16_f32 v165, v166, v167\n"
      "ds_write_b64 v89, v[164:165] offset:32\n"
      "v_mul_f32_e32 v168, v16, v168\n"
      "v_mfma_f32_16x16x32_bf16 v[200:203], a[168:171], v[40:43], v[200:203]\n"
      "v_add_f32_e32 v18, v33, v18\n"
      "v_cvt_pk_bf16_f32 v44, v32, v33\n"
      "v_mul_f32_e32 v169, v16, v169\n"
      "v_mul_f32_e32 v170, v16, v170\n"
      "v_mul_f32_e32 v171, v16, v171\n"
      "v_mfma_f32_16x16x32_bf16 v[204:207], a[172:175], v[40:43], v[204:207]\n"
      "ds_read_b128 a[144:147], v1 offset:12288\n"
      "v_exp_f32_e32 v34, v34\n"
      "v_cvt_pk_bf16_f32 v168, v168, v169\n"
      "v_cvt_pk_bf16_f32 v169, v170, v171\n"
      "ds_write_b64 v89, v[168:169] offset:64\n"
      "v_mfma_f32_16x16x32_bf16 v[208:211], a[176:179], v[40:43], v[208:211]\n"
      "ds_read_b128 a[148:151], v2 offset:12288\n"
      "ds_read_b128 a[152:155], v1 offset:13312\n"
      "v_add_f32_e32 v18, v34, v18\n"
      "v_mul_f32_e32 v172, v16, v172\n"
      "v_mul_f32_e32 v173, v16, v173\n"
      "v_mul_f32_e32 v174, v16, v174\n"
      "v_mfma_f32_16x16x32_bf16 v[212:215], a[180:183], v[40:43], v[212:215]\n"
      "ds_read_b128 a[156:159], v2 offset:13312\n"
      "v_exp_f32_e32 v35, v35\n"
      "s_waitcnt lgkmcnt(6)\n"
      "buffer_store_dwordx4 v[148:151], v88, s[28:31], s39 offen offset:1024 sc1 nt\n"
      "v_mul_f32_e32 v175, v16, v175\n"
      "v_cvt_pk_bf16_f32 v172, v172, v173\n"
      "v_cvt_pk_bf16_f32 v173, v174, v175\n"
      "v_mfma_f32_16x16x32_bf16 v[216:219], a[184:187], v[40:43], v[216:219]\n"
      "v_add_f32_e32 v18, v35, v18\n"
      "v_cvt_pk_bf16_f32 v45, v34, v35\n"
      "ds_write_b64 v89, v[172:173] offset:96\n"
      "v_mul_f32_e32 v176, v16, v176\n"
      "v_mul_f32_e32 v177, v16, v177\n"
      "v_mfma_f32_16x16x32_bf16 v[220:223], a[188:191], v[40:43], v[220:223]\n"
      "v_exp_f32_e32 v36, v36\n"
      "v_mul_f32_e32 v178, v16, v178\n"
      "v_mul_f32_e32 v179, v16, v179\n"
      "v_cvt_pk_bf16_f32 v176, v176, v177\n"
      "v_mfma_f32_16x16x32_bf16 v[24:27], a[128:131], a[64:67], v[64:67]\n"
      "ds_read_b64_tr_b16 a[192:193], v3 offset:8192\n"
      "ds_read_b64_tr_b16 a[194:195], v3 offset:12288\n"
      "v_add_f32_e32 v18, v36, v18\n"
      "v_cvt_pk_bf16_f32 v177, v178, v179\n"
      "ds_write_b64 v89, v[176:177] offset:128\n"
      "v_mul_f32_e32 v180, v16, v180\n"
      "v_mfma_f32_16x16x32_bf16 v[24:27], a[132:135], a[68:71], v[24:27]\n"
      "v_exp_f32_e32 v37, v37\n"
      "v_mul_f32_e32 v181, v16, v181\n"
      "v_mul_f32_e32 v182, v16, v182\n"
      "v_mul_f32_e32 v183, v16, v183\n"
      "v_mfma_f32_16x16x32_bf16 v[24:27], a[136:139], a[72:75], v[24:27]\n"
      "v_add_f32_e32 v18, v37, v18\n"
      "v_cvt_pk_bf16_f32 v46, v36, v37\n"
      "v_cvt_pk_bf16_f32 v180, v180, v181\n"
      "v_cvt_pk_bf16_f32 v181, v182, v183\n"
      "ds_write_b64 v89, v[180:181] offset:160\n"
      "v_mfma_f32_16x16x32_bf16 v[24:27], a[140:143], a[76:79], v[24:27]\n"
      "v_exp_f32_e32 v38, v38\n"
      "v_mul_f32_e32 v184, v16, v184\n"
      "v_mul_f32_e32 v185, v16, v185\n"
      "v_mul_f32_e32 v186, v16, v186\n"
      "s_waitcnt lgkmcnt(4)\n"
      "v_mfma_f32_16x16x32_bf16 v[28:31], a[144:147], a[64:67], v[64:67]\n"
      "ds_read_b64_tr_b16 a[196:197], v4 offset:8192\n"
      "ds_read_b64_tr_b16 a[198:199], v4 offset:12288\n"
      "v_add_f32_e32 v18, v38, v18\n"
      "v_mul_f32_e32 v187, v16, v187\n"
      "v_cvt_pk_bf16_f32 v184, v184, v185\n"
      "v_cvt_pk_bf16_f32 v185, v186, v187\n"
      "v_mfma_f32_16x16x32_bf16 v[28:31], a[148:151], a[68:71], v[28:31]\n"
      "v_exp_f32_e32 v39, v39\n"
      "ds_write_b64 v89, v[184:185] offset:192\n"
      "v_mul_f32_e32 v188, v16, v188\n"
      "v_mul_f32_e32 v189, v16, v189\n"
      "v_mfma_f32_16x16x32_bf16 v[28:31], a[152:155], a[72:75], v[28:31]\n"
      "v_add_f32_e32 v18, v39, v18\n"
      "v_cvt_pk_bf16_f32 v47, v38, v39\n"
      "v_mul_f32_e32 v190, v16, v190\n"
      "v_mul_f32_e32 v191, v16, v191\n"
      "v_cvt_pk_bf16_f32 v188, v188, v189\n"
      "v_mfma_f32_16x16x32_bf16 v[28:31], a[156:159], a[76:79], v[28:31]\n"
      "v_exp_f32_e32 v24, v24\n"
      "buffer_store_dwordx4 v[152:155], v88, s[28:31], s39 offen offset:2048 sc1 nt\n"
      "v_cvt_pk_bf16_f32 v189, v190, v191\n"
      "ds_write_b64 v89, v[188:189] offset:224\n"
      "ds_read_b128 v[176:179], v90 offset:0\n"
      "ds_read_b128 v[180:183], v90 offset:1088\n"
      "ds_read_b128 v[184:187], v90 offset:2176\n"
      "ds_read_b128 v[188:191], v90 offset:3264\n"
      "v_mfma_f32_16x16x32_bf16 v[224:227], a[160:163], v[44:47], v[224:227]\n"
      "ds_read_b64_tr_b16 a[200:201], v5 offset:8192\n"
      "ds_read_b64_tr_b16 a[202:203], v5 offset:12288\n"
      "v_add_f32_e32 v17, v24, v17\n"
      "v_mfma_f32_16x16x32_bf16 v[228:231], a[164:167], v[44:47], v[228:231]\n"
      "ds_read_b64_tr_b16 a[204:205], v6 offset:8192\n"
      "v_exp_f32_e32 v25, v25\n"
      "v_mfma_f32_16x16x32_bf16 v[232:235], a[168:171], v[44:47], v[232:235]\n"
      "v_add_f32_e32 v17, v25, v17\n"
      "v_cvt_pk_bf16_f32 v40, v24, v25\n"
      "v_mfma_f32_16x16x32_bf16 v[236:239], a[172:175], v[44:47], v[236:239]\n"
      "ds_read_b64_tr_b16 a[206:207], v6 offset:12288\n"
      "v_exp_f32_e32 v26, v26\n"
      "v_mfma_f32_16x16x32_bf16 v[240:243], a[176:179], v[44:47], v[240:243]\n"
      "ds_read_b64_tr_b16 a[208:209], v3 offset:9216\n"
      "ds_read_b64_tr_b16 a[210:211], v3 offset:13312\n"
      "v_add_f32_e32 v17, v26, v17\n"
      "v_mfma_f32_16x16x32_bf16 v[244:247], a[180:183], v[44:47], v[244:247]\n"
      "ds_read_b64_tr_b16 a[212:213], v4 offset:9216\n"
      "v_exp_f32_e32 v27, v27\n"
      "v_mfma_f32_16x16x32_bf16 v[248:251], a[184:187], v[44:47], v[248:251]\n"
      "v_add_f32_e32 v17, v27, v17\n"
      "v_cvt_pk_bf16_f32 v41, v26, v27\n"
      "v_mfma_f32_16x16x32_bf16 v[252:255], a[188:191], v[44:47], v[252:255]\n"
      "ds_read_b64_tr_b16 a[214:215], v4 offset:13312\n"
      "v_exp_f32_e32 v28, v28\n"
      "v_mfma_f32_16x16x32_bf16 v[32:35], a[128:131], a[80:83], v[68:71]\n"
      "ds_read_b64_tr_b16 a[216:217], v5 offset:9216\n"
      "ds_read_b64_tr_b16 a[218:219], v5 offset:13312\n"
      "v_add_f32_e32 v17, v28, v17\n"
      "v_mfma_f32_16x16x32_bf16 v[32:35], a[132:135], a[84:87], v[32:35]\n"
      "ds_read_b64_tr_b16 a[220:221], v6 offset:9216\n"
      "v_exp_f32_e32 v29, v29\n"
      "buffer_store_dwordx4 v[156:159], v88, s[28:31], s39 offen offset:3072 sc1 nt\n"
      "v_mfma_f32_16x16x32_bf16 v[32:35], a[136:139], a[88:91], v[32:35]\n"
      "v_add_f32_e32 v17, v29, v17\n"
      "v_cvt_pk_bf16_f32 v42, v28, v29\n"
      "v_mfma_f32_16x16x32_bf16 v[32:35], a[140:143], a[92:95], v[32:35]\n"
      "ds_read_b64_tr_b16 a[222:223], v6 offset:13312\n"
      "v_exp_f32_e32 v30, v30\n"
      "v_mfma_f32_16x16x32_bf16 v[36:39], a[144:147], a[80:83], v[68:71]\n"
      "ds_read_b128 a[96:99], v1 offset:16384\n"
      "ds_read_b128 a[100:103], v2 offset:16384\n"
      "v_add_f32_e32 v17, v30, v17\n"
      "v_mfma_f32_16x16x32_bf16 v[36:39], a[148:151], a[84:87], v[36:39]\n"
      "v_exp_f32_e32 v31, v31\n"
      "v_mfma_f32_16x16x32_bf16 v[36:39], a[152:155], a[88:91], v[36:39]\n"
      "v_add_f32_e32 v17, v31, v17\n"
      "v_cvt_pk_bf16_f32 v43, v30, v31\n"
      "v_mfma_f32_16x16x32_bf16 v[36:39], a[156:159], a[92:95], v[36:39]\n"
      "v_exp_f32_e32 v32, v32\n"
      "s_waitcnt lgkmcnt(2)\n"
      "v_mfma_f32_16x16x32_bf16 v[192:195], a[192:195], v[40:43], v[192:195]\n"
      "ds_read_b128 a[104:107], v1 offset:17408\n"
      "ds_read_b128 a[108:111], v2 offset:17408\n"
      "v_add_f32_e32 v18, v32, v18\n"
      "v_mfma_f32_16x16x32_bf16 v[196:199], a[196:199], v[40:43], v[196:199]\n"
      "v_exp_f32_e32 v33, v33\n"
      "v_mfma_f32_16x16x32_bf16 v[200:203], a[200:203], v[40:43], v[200:203]\n"
      "v_add_f32_e32 v18, v33, v18\n"
      "v_cvt_pk_bf16_f32 v44, v32, v33\n"
      "v_mfma_f32_16x16x32_bf16 v[204:207], a[204:207], v[40:43], v[204:207]\n"
      "ds_read_b128 a[112:115], v1 offset:20480\n"
      "v_exp_f32_e32 v34, v34\n"
      "s_mov_b32 s39, 12288\n"
      "buffer_store_dwordx4 v[176:179], v88, s[28:31], s39 offen sc1 nt\n"
      "v_mfma_f32_16x16x32_bf16 v[208:211], a[208:211], v[40:43], v[208:211]\n"
      "ds_read_b128 a[116:119], v2 offset:20480\n"
      "ds_read_b128 a[120:123], v1 offset:21504\n"
      "v_add_f32_e32 v18, v34, v18\n"
      "v_mfma_f32_16x16x32_bf16 v[212:215], a[212:215], v[40:43], v[212:215]\n"
      "ds_read_b128 a[124:127], v2 offset:21504\n"
      "v_exp_f32_e32 v35, v35\n"
      "v_mfma_f32_16x16x32_bf16 v[216:219], a[216:219], v[40:43], v[216:219]\n"
      "v_add_f32_e32 v18, v35, v18\n"
      "v_cvt_pk_bf16_f32 v45, v34, v35\n"
      "v_mfma_f32_16x16x32_bf16 v[220:223], a[220:223], v[40:43], v[220:223]\n"
      "v_exp_f32_e32 v36, v36\n"
      "s_waitcnt lgkmcnt(3)\n"
      "v_mfma_f32_16x16x32_bf16 v[24:27], a[96:99], a[64:67], v[64:67]\n"
      "ds_read_b64_tr_b16 a[160:161], v3 offset:16384\n"
      "ds_read_b64_tr_b16 a[162:163], v3 offset:20480\n"
      "v_add_f32_e32 v18, v36, v18\n"
      "v_mfma_f32_16x16x32_bf16 v[24:27], a[100:103], a[68:71], v[24:27]\n"
      "v_exp_f32_e32 v37, v37\n"
      "v_mfma_f32_16x16x32_bf16 v[24:27], a[104:107], a[72:75], v[24:27]\n"
      "v_add_f32_e32 v18, v37, v18\n"
      "v_cvt_pk_bf16_f32 v46, v36, v37\n"
      "v_mfma_f32_16x16x32_bf16 v[24:27], a[108:111], a[76:79], v[24:27]\n"
      "v_exp_f32_e32 v38, v38\n"
      "v_mfma_f32_16x16x32_bf16 v[28:31], a[112:115], a[64:67], v[64:67]\n"
      "ds_read_b64_tr_b16 a[164:165], v4 offset:16384\n"
      "ds_read_b64_tr_b16 a[166:167], v4 offset:20480\n"
      "v_add_f32_e32 v18, v38, v18\n"
      "s_waitcnt lgkmcnt(2)\n"
      "v_mfma_f32_16x16x32_bf16 v[28:31], a[116:119], a[68:71], v[28:31]\n"
      "v_exp_f32_e32 v39, v39\n"
      "buffer_store_dwordx4 v[180:183], v88, s[28:31], s39 offen offset:1024 sc1 nt\n"
      "v_mfma_f32_16x16x32_bf16 v[28:31], a[120:123], a[72:75], v[28:31]\n"
      "v_add_f32_e32 v18, v39, v18\n"
      "v_cvt_pk_bf16_f32 v47, v38, v39\n"
      "v_mfma_f32_16x16x32_bf16 v[28:31], a[124:127], a[76:79], v[28:31]\n"
      "v_exp_f32_e32 v24, v24\n"
      "v_mfma_f32_16x16x32_bf16 v[224:227], a[192:195], v[44:47], v[224:227]\n"
      "ds_read_b64_tr_b16 a[168:169], v5 offset:16384\n"
      "ds_read_b64_tr_b16 a[170:171], v5 offset:20480\n"
      "v_add_f32_e32 v17, v24, v17\n"
      "v_mfma_f32_16x16x32_bf16 v[228:231], a[196:199], v[44:47], v[228:231]\n"
      "ds_read_b64_tr_b16 a[172:173], v6 offset:16384\n"
      "v_exp_f32_e32 v25, v25\n"
      "v_mfma_f32_16x16x32_bf16 v[232:235], a[200:203], v[44:47], v[232:235]\n"
      "v_add_f32_e32 v17, v25, v17\n"
      "v_cvt_pk_bf16_f32 v40, v24, v25\n"
      "v_mfma_f32_16x16x32_bf16 v[236:239], a[204:207], v[44:47], v[236:239]\n"
      "ds_read_b64_tr_b16 a[174:175], v6 offset:20480\n"
      "v_exp_f32_e32 v26, v26\n"
      "v_mfma_f32_16x16x32_bf16 v[240:243], a[208:211], v[44:47], v[240:243]\n"
      "ds_read_b64_tr_b16 a[176:177], v3 offset:17408\n"
      "ds_read_b64_tr_b16 a[178:179], v3 offset:21504\n"
      "v_add_f32_e32 v17, v26, v17\n"
      "v_mfma_f32_16x16x32_bf16 v[244:247], a[212:215], v[44:47], v[244:247]\n"
      "ds_read_b64_tr_b16 a[180:181], v4 offset:17408\n"
      "v_exp_f32_e32 v27, v27\n"
      "v_mfma_f32_16x16x32_bf16 v[248:251], a[216:219], v[44:47], v[248:251]\n"
      "v_add_f32_e32 v17, v27, v17\n"
      "v_cvt_pk_bf16_f32 v41, v26, v27\n"
      "v_mfma_f32_16x16x32_bf16 v[252:255], a[220:223], v[44:47], v[252:255]\n"
      "ds_read_b64_tr_b16 a[182:183], v4 offset:21504\n"
      "v_exp_f32_e32 v28, v28\n"
      "buffer_store_dwordx4 v[184:187], v88, s[28:31], s39 offen offset:2048 sc1 nt\n"
      "v_mfma_f32_16x16x32_bf16 v[32:35], a[96:99], a[80:83], v[68:71]\n"
      "ds_read_b64_tr_b16 a[184:185], v5 offset:17408\n"
      "ds_read_b64_tr_b16 a[186:187], v5 offset:21504\n"
      "v_add_f32_e32 v17, v28, v17\n"
      "v_mfma_f32_16x16x32_bf16 v[32:35], a[100:103], a[84:87], v[32:35]\n"
      "ds_read_b64_tr_b16 a[188:189], v6 offset:17408\n"
      "v_exp_f32_e32 v29, v29\n"
      "v_mfma_f32_16x16x32_bf16 v[32:35], a[104:107], a[88:91], v[32:35]\n"
      "v_add_f32_e32 v17, v29, v17\n"
      "v_cvt_pk_bf16_f32 v42, v28, v29\n"
      "v_mfma_f32_16x16x32_bf16 v[32:35], a[108:111], a[92:95], v[32:35]\n"
      "ds_read_b64_tr_b16 a[190:191], v6 offset:21504\n"
      "v_exp_f32_e32 v30, v30\n"
      "v_mfma_f32_16x16x32_bf16 v[36:39], a[112:115], a[80:83], v[68:71]\n"
      "ds_read_b128 a[128:131], v1 offset:24576\n"
      "ds_read_b128 a[132:135], v2 offset:24576\n"
      "v_add_f32_e32 v17, v30, v17\n"
      "v_mfma_f32_16x16x32_bf16 v[36:39], a[116:119], a[84:87], v[36:39]\n"
      "v_exp_f32_e32 v31, v31\n"
      "v_mfma_f32_16x16x32_bf16 v[36:39], a[120:123], a[88:91], v[36:39]\n"
      "v_add_f32_e32 v17, v31, v17\n"
      "v_cvt_pk_bf16_f32 v43, v30, v31\n"
      "v_mfma_f32_16x16x32_bf16 v[36:39], a[124:127], a[92:95], v[36:39]\n"
      "v_exp_f32_e32 v32, v32\n"
      "v_mfma_f32_16x16x32_bf16 v[192:195], a[160:163], v[40:43], v[192:195]\n"
      "ds_read_b128 a[136:139], v1 offset:25600\n"
      "ds_read_b128 a[140:143], v2 offset:25600\n"
      "v_add_f32_e32 v18, v32, v18\n"
      "s_waitcnt lgkmcnt(2)\n"
      "v_mfma_f32_16x16x32_bf16 v[196:199], a[164:167], v[40:43], v[196:199]\n"
      "v_exp_f32_e32 v33, v33\n"
      "buffer_store_dwordx4 v[188:191], v88, s[28:31], s39 offen offset:3072 sc1 nt\n"
      "v_mfma_f32_16x16x32_bf16 v[200:203], a[168:171], v[40:43], v[200:203]\n"
      "v_add_f32_e32 v18, v33, v18\n"
      "v_cvt_pk_bf16_f32 v44, v32, v33\n"
      "v_mfma_f32_16x16x32_bf16 v[204:207], a[172:175], v[40:43], v[204:207]\n"
      "ds_read_b128 a[144:147], v1 offset:28672\n"
      "v_exp_f32_e32 v34, v34\n"
      "v_mfma_f32_16x16x32_bf16 v[208:211], a[176:179], v[40:43], v[208:211]\n"
      "ds_read_b128 a[148:151], v2 offset:28672\n"
      "ds_read_b128 a[152:155], v1 offset:29696\n"
      "v_add_f32_e32 v18, v34, v18\n"
      "v_mfma_f32_16x16x32_bf16 v[212:215], a[180:183], v[40:43], v[212:215]\n"
      "ds_read_b128 a[156:159], v2 offset:29696\n"
      "v_exp_f32_e32 v35, v35\n"
      "v_mfma_f32_16x16x32_bf16 v[216:219], a[184:187], v[40:43], v[216:219]\n"
      "v_add_f32_e32 v18, v35, v18\n"
      "v_cvt_pk_bf16_f32 v45, v34, v35\n"
      "v_mfma_f32_16x16x32_bf16 v[220:223], a[188:191], v[40:43], v[220:223]\n"
      "v_exp_f32_e32 v36, v36\n"
      "v_mfma_f32_16x16x32_bf16 v[24:27], a[128:131], a[64:67], v[64:67]\n"
      "ds_read_b64_tr_b16 a[192:193], v3 offset:24576\n"
      "ds_read_b64_tr_b16 a[194:195], v3 offset:28672\n"
      "v_add_f32_e32 v18, v36, v18\n"
      "v_mfma_f32_16x16x32_bf16 v[24:27], a[132:135], a[68:71], v[24:27]\n"
      "v_exp_f32_e32 v37, v37\n"
      "s_waitcnt lgkmcnt(2)\n"
      "v_mfma_f32_16x16x32_bf16 v[24:27], a[136:139], a[72:75], v[24:27]\n"
      "v_add_f32_e32 v18, v37, v18\n"
      "v_cvt_pk_bf16_f32 v46, v36, v37\n"
      "v_mfma_f32_16x16x32_bf16 v[24:27], a[140:143], a[76:79], v[24:27]\n"
      "v_exp_f32_e32 v38, v38\n"
      "v_mfma_f32_16x16x32_bf16 v[28:31], a[144:147], a[64:67], v[64:67]\n"
      "ds_read_b64_tr_b16 a[196:197], v4 offset:24576\n"
      "ds_read_b64_tr_b16 a[198:199], v4 offset:28672\n"
      "v_add_f32_e32 v18, v38, v18\n"
      "v_mfma_f32_16x16x32_bf16 v[28:31], a[148:151], a[68:71], v[28:31]\n"
      "v_exp_f32_e32 v39, v39\n"
      "v_mfma_f32_16x16x32_bf16 v[28:31], a[152:155], a[72:75], v[28:31]\n"
      "v_add_f32_e32 v18, v39, v18\n"
      "v_cvt_pk_bf16_f32 v47, v38, v39\n"
      "v_mfma_f32_16x16x32_bf16 v[28:31], a[156:159], a[76:79], v[28:31]\n"
      "v_exp_f32_e32 v24, v24\n"
      "v_mfma_f32_16x16x32_bf16 v[224:227], a[160:163], v[44:47], v[224:227]\n"
      "ds_read_b64_tr_b16 a[200:201], v5 offset:24576\n"
      "ds_read_b64_tr_b16 a[202:203], v5 offset:28672\n"
      "v_add_f32_e32 v17, v24, v17\n"
      "v_mfma_f32_16x16x32_bf16 v[228:231], a[164:167], v[44:47], v[228:231]\n"
      "ds_read_b64_tr_b16 a[204:205], v6 offset:24576\n"
      "v_exp_f32_e32 v25, v25\n"
      "v_mfma_f32_16x16x32_bf16 v[232:235], a[168:171], v[44:47], v[232:235]\n"
      "v_add_f32_e32 v17, v25, v17\n"
      "v_cvt_pk_bf16_f32 v40, v24, v25\n"
      "v_mfma_f32_16x16x32_bf16 v[236:239], a[172:175], v[44:47], v[236:239]\n"
      "ds_read_b64_tr_b16 a[206:207], v6 offset:28672\n"
      "v_exp_f32_e32 v26, v26\n"
      "v_mfma_f32_16x16x32_bf16 v[240:243], a[176:179], v[44:47], v[240:243]\n"
      "ds_read_b64_tr_b16 a[208:209], v3 offset:25600\n"
      "ds_read_b64_tr_b16 a[210:211], v3 offset:29696\n"
      "v_add_f32_e32 v17, v26, v17\n"
      "v_mfma_f32_16x16x32_bf16 v[244:247], a[180:183], v[44:47], v[244:247]\n"
      "ds_read_b64_tr_b16 a[212:213], v4 offset:25600\n"
      "v_exp_f32_e32 v27, v27\n"
      "v_mfma_f32_16x16x32_bf16 v[248:251], a[184:187], v[44:47], v[248:251]\n"
      "v_add_f32_e32 v17, v27, v17\n"
      "v_cvt_pk_bf16_f32 v41, v26, v27\n"
      "v_mfma_f32_16x16x32_bf16 v[252:255], a[188:191], v[44:47], v[252:255]\n"
      "ds_read_b64_tr_b16 a[214:215], v4 offset:29696\n"
      "v_exp_f32_e32 v28, v28\n"
      "v_mfma_f32_16x16x32_bf16 v[32:35], a[128:131], a[80:83], v[68:71]\n"
      "ds_read_b64_tr_b16 a[216:217], v5 offset:25600\n"
      "ds_read_b64_tr_b16 a[218:219], v5 offset:29696\n"
      "v_add_f32_e32 v17, v28, v17\n"
      "v_mfma_f32_16x16x32_bf16 v[32:35], a[132:135], a[84:87], v[32:35]\n"
      "ds_read_b64_tr_b16 a[220:221], v6 offset:25600\n"
      "v_exp_f32_e32 v29, v29\n"
      "v_mfma_f32_16x16x32_bf16 v[32:35], a[136:139], a[88:91], v[32:35]\n"
      "v_add_f32_e32 v17, v29, v17\n"
      "v_cvt_pk_bf16_f32 v42, v28, v29\n"
      "v_mfma_f32_16x16x32_bf16 v[32:35], a[140:143], a[92:95], v[32:35]\n"
      "ds_read_b64_tr_b16 a[222:223], v6 offset:29696\n"
      "v_exp_f32_e32 v30, v30\n"
      "v_mfma_f32_16x16x32_bf16 v[36:39], a[144:147], a[80:83], v[68:71]\n"
      "v_add_f32_e32 v17, v30, v17\n"
      "v_mfma_f32_16x16x32_bf16 v[36:39], a[148:151], a[84:87], v[36:39]\n"
      "v_exp_f32_e32 v31, v31\n"
      "v_mfma_f32_16x16x32_bf16 v[36:39], a[152:155], a[88:91], v[36:39]\n"
      "v_add_f32_e32 v17, v31, v17\n"
      "v_cvt_pk_bf16_f32 v43, v30, v31\n"
      "v_mfma_f32_16x16x32_bf16 v[36:39], a[156:159], a[92:95], v[36:39]\n"
      "v_exp_f32_e32 v32, v32\n"
      "s_waitcnt lgkmcnt(0)\n"
      "v_mfma_f32_16x16x32_bf16 v[192:195], a[192:195], v[40:43], v[192:195]\n"
      "v_add_f32_e32 v18, v32, v18\n"
      "v_mov_b32_e32 v23, v17\n"
      "s_nop 1\n"
      "v_permlane16_swap_b32_e32 v23, v17\n"
      "v_mfma_f32_16x16x32_bf16 v[196:199], a[196:199], v[40:43], v[196:199]\n"
      "v_exp_f32_e32 v33, v33\n"
      "v_add_f32_e32 v17, v23, v17\n"
      "v_mov_b32_e32 v23, v17\n"
      "v_mfma_f32_16x16x32_bf16 v[200:203], a[200:203], v[40:43], v[200:203]\n"
      "v_add_f32_e32 v18, v33, v18\n"
      "v_cvt_pk_bf16_f32 v44, v32, v33\n"
      "v_permlane32_swap_b32_e32 v23, v17\n"
      "v_add_f32_e32 v17, v23, v17\n"
      "v_mfma_f32_16x16x32_bf16 v[204:207], a[204:207], v[40:43], v[204:207]\n"
      "v_exp_f32_e32 v34, v34\n"
      "v_cmp_nge_f32_e32 vcc, s46, v17\n"
      "v_rcp_f32_e32 v17, v17\n"
      "v_mfma_f32_16x16x32_bf16 v[208:211], a[208:211], v[40:43], v[208:211]\n"
      "v_add_f32_e32 v18, v34, v18\n"
      "s_or_b64 s[44:45], s[44:45], vcc\n"
      "v_mfma_f32_16x16x32_bf16 v[212:215], a[212:215], v[40:43], v[212:215]\n"
      "v_exp_f32_e32 v35, v35\n"
      "v_mul_f32_e32 v192, v17, v192\n"
      "v_mul_f32_e32 v193, v17, v193\n"
      "v_mul_f32_e32 v194, v17, v194\n"
      "v_mul_f32_e32 v195, v17, v195\n"
      "v_cvt_pk_bf16_f32 v192, v192, v193\n"
      "v_cvt_pk_bf16_f32 v193, v194, v195\n"
      "ds_write_b64 v89, v[192:193] offset:0\n"
      "v_mul_f32_e32 v196, v17, v196\n"
      "v_mfma_f32_16x16x32_bf16 v[216:219], a[216:219], v[40:43], v[216:219]\n"
      "v_add_f32_e32 v18, v35, v18\n"
      "v_cvt_pk_bf16_f32 v45, v34, v35\n"
      "v_mul_f32_e32 v197, v17, v197\n"
      "v_mul_f32_e32 v198, v17, v198\n"
      "v_mul_f32_e32 v199, v17, v199\n"
      "v_cvt_pk_bf16_f32 v196, v196, v197\n"
      "v_cvt_pk_bf16_f32 v197, v198, v199\n"
      "ds_write_b64 v89, v[196:197] offset:32\n"
      "v_mul_f32_e32 v200, v17, v200\n"
      "v_mul_f32_e32 v201, v17, v201\n"
      "v_mfma_f32_16x16x32_bf16 v[220:223], a[220:223], v[40:43], v[220:223]\n"
      "v_exp_f32_e32 v36, v36\n"
      "v_mul_f32_e32 v202, v17, v202\n"
      "v_mul_f32_e32 v203, v17, v203\n"
      "v_cvt_pk_bf16_f32 v200, v200, v201\n"
      "v_cvt_pk_bf16_f32 v201, v202, v203\n"
      "ds_write_b64 v89, v[200:201] offset:64\n"
      "v_mul_f32_e32 v204, v17, v204\n"
      "v_mul_f32_e32 v205, v17, v205\n"
      "v_mul_f32_e32 v206, v17, v206\n"
      "v_add_f32_e32 v18, v36, v18\n"
      "v_mul_f32_e32 v207, v17, v207\n"
      "v_cvt_pk_bf16_f32 v204, v204, v205\n"
      "v_cvt_pk_bf16_f32 v205, v206, v207\n"
      "ds_write_b64 v89, v[204:205] offset:96\n"
      "v_mul_f32_e32 v208, v17, v208\n"
      "v_mul_f32_e32 v209, v17, v209\n"
      "v_mul_f32_e32 v210, v17, v210\n"
      "v_mul_f32_e32 v211, v17, v211\n"
      "v_exp_f32_e32 v37, v37\n"
      "v_cvt_pk_bf16_f32 v208, v208, v209\n"
      "v_cvt_pk_bf16_f32 v209, v210, v211\n"
      "ds_write_b64 v89, v[208:209] offset:128\n"
      "v_mul_f32_e32 v212, v17, v212\n"
      "v_mul_f32_e32 v213, v17, v213\n"
      "v_mul_f32_e32 v214, v17, v214\n"
      "v_mul_f32_e32 v215, v17, v215\n"
      "v_cvt_pk_bf16_f32 v212, v212, v213\n"
      "v_add_f32_e32 v18, v37, v18\n"
      "v_cvt_pk_bf16_f32 v46, v36, v37\n"
      "v_cvt_pk_bf16_f32 v213, v214, v215\n"
      "ds_write_b64 v89, v[212:213] offset:160\n"
      "v_mul_f32_e32 v216, v17, v216\n"
      "v_mul_f32_e32 v217, v17, v217\n"
      "v_mul_f32_e32 v218, v17, v218\n"
      "v_mul_f32_e32 v219, v17, v219\n"
      "v_cvt_pk_bf16_f32 v216, v216, v217\n"
      "v_cvt_pk_bf16_f32 v217, v218, v219\n"
      "v_exp_f32_e32 v38, v38\n"
      "ds_write_b64 v89, v[216:217] offset:192\n"
      "v_mul_f32_e32 v220, v17, v220\n"
      "v_mul_f32_e32 v221, v17, v221\n"
      "v_mul_f32_e32 v222, v17, v222\n"
      "v_mul_f32_e32 v223, v17, v223\n"
      "v_cvt_pk_bf16_f32 v220, v220, v221\n"
      "v_cvt_pk_bf16_f32 v221, v222, v223\n"
      "ds_write_b64 v89, v[220:221] offset:224\n"
      "v_add_f32_e32 v18, v38, v18\n"
      "ds_read_b128 v[208:211], v90 offset:0\n"
      "ds_read_b128 v[212:215], v90 offset:1088\n"
      "ds_read_b128 v[216:219], v90 offset:2176\n"
      "ds_read_b128 v[220:223], v90 offset:3264\n"
      "v_exp_f32_e32 v39, v39\n"
      "s_mov_b32 s39, 16384\n"
      "s_waitcnt lgkmcnt(3)\n"
      "buffer_store_dwordx4 v[208:211], v88, s[28:31], s39 offen sc1 nt\n"
      "v_add_f32_e32 v18, v39, v18\n"
      "v_cvt_pk_bf16_f32 v47, v38, v39\n"
      "s_waitcnt lgkmcnt(2)\n"
      "buffer_store_dwordx4 v[212:215], v88, s[28:31], s39 offen offset:1024 sc1 nt\n"
      "s_waitcnt lgkmcnt(1)\n"
      "buffer_store_dwordx4 v[216:219], v88, s[28:31], s39 offen offset:2048 sc1 nt\n"
      "v_mfma_f32_16x16x32_bf16 v[224:227], a[192:195], v[44:47], v[224:227]\n"
      "s_waitcnt lgkmcnt(0)\n"
      "buffer_store_dwordx4 v[220:223], v88, s[28:31], s39 offen offset:3072 sc1 nt\n"
      "v_mov_b32_e32 v23, v18\n"
      "s_nop 1\n"
      "v_permlane16_swap_b32_e32 v23, v18\n"
      "v_mfma_f32_16x16x32_bf16 v[228:231], a[196:199], v[44:47], v[228:231]\n"
      "v_add_f32_e32 v18, v23, v18\n"
      "v_mov_b32_e32 v23, v18\n"
      "v_mfma_f32_16x16x32_bf16 v[232:235], a[200:203], v[44:47], v[232:235]\n"
      "s_nop 0\n"
      "v_permlane32_swap_b32_e32 v23, v18\n"
      "v_add_f32_e32 v18, v23, v18\n"
      "v_mfma_f32_16x16x32_bf16 v[236:239], a[204:207], v[44:47], v[236:239]\n"
      "v_cmp_nge_f32_e32 vcc, s46, v18\n"
      "v_rcp_f32_e32 v18, v18\n"
      "v_mfma_f32_16x16x32_bf16 v[240:243], a[208:211], v[44:47], v[240:243]\n"
      "s_or_b64 s[44:45], s[44:45], vcc\n"
      "v_mfma_f32_16x16x32_bf16 v[244:247], a[212:215], v[44:47], v[244:247]\n"
      "v_mul_f32_e32 v224, v18, v224\n"
      "v_mul_f32_e32 v225, v18, v225\n"
      "v_mul_f32_e32 v226, v18, v226\n"
      "v_mul_f32_e32 v227, v18, v227\n"
      "v_cvt_pk_bf16_f32 v224, v224, v225\n"
      "v_cvt_pk_bf16_f32 v225, v226, v227\n"
      "ds_write_b64 v89, v[224:225] offset:0\n"
      "v_mul_f32_e32 v228, v18, v228\n"
      "v_mfma_f32_16x16x32_bf16 v[248:251], a[216:219], v[44:47], v[248:251]\n"
      "v_mul_f32_e32 v229, v18, v229\n"
      "v_mul_f32_e32 v230, v18, v230\n"
      "v_mul_f32_e32 v231, v18, v231\n"
      "v_cvt_pk_bf16_f32 v228, v228, v229\n"
      "v_cvt_pk_bf16_f32 v229, v230, v231\n"
      "ds_write_b64 v89, v[228:229] offset:32\n"
      "v_mul_f32_e32 v232, v18, v232\n"
      "v_mul_f32_e32 v233, v18, v233\n"
      "v_mfma_f32_16x16x32_bf16 v[252:255], a[220:223], v[44:47], v[252:255]\n"
      "v_mul_f32_e32 v234, v18, v234\n"
      "v_mul_f32_e32 v235, v18, v235\n"
      "v_cvt_pk_bf16_f32 v232, v232, v233\n"
      "v_cvt_pk_bf16_f32 v233, v234, v235\n"
      "ds_write_b64 v89, v[232:233] offset:64\n"
      "v_mul_f32_e32 v236, v18, v236\n"
      "v_mul_f32_e32 v237, v18, v237\n"
      "v_mul_f32_e32 v238, v18, v238\n"
      "v_mul_f32_e32 v239, v18, v239\n"
      "v_cvt_pk_bf16_f32 v236, v236, v237\n"
      "v_cvt_pk_bf16_f32 v237, v238, v239\n"
      "ds_write_b64 v89, v[236:237] offset:96\n"
      "v_pk_mul_f32 v[240:241], v[240:241], v[18:19] op_sel_hi:[1,0]\n"
      "v_pk_mul_f32 v[242:243], v[242:243], v[18:19] op_sel_hi:[1,0]\n"
      "v_cvt_pk_bf16_f32 v240, v240, v241\n"
      "v_cvt_pk_bf16_f32 v241, v242, v243\n"
      "ds_write_b64 v89, v[240:241] offset:128\n"
      "v_pk_mul_f32 v[244:245], v[244:245], v[18:19] op_sel_hi:[1,0]\n"
      "v_pk_mul_f32 v[246:247], v[246:247], v[18:19] op_sel_hi:[1,0]\n"
      "v_cvt_pk_bf16_f32 v244, v244, v245\n"
      "v_cvt_pk_bf16_f32 v245, v246, v247\n"
      "ds_write_b64 v89, v[244:245] offset:160\n"
      "v_pk_mul_f32 v[248:249], v[248:249], v[18:19] op_sel_hi:[1,0]\n"
      "v_pk_mul_f32 v[250:251], v[250:251], v[18:19] op_sel_hi:[1,0]\n"
      "v_cvt_pk_bf16_f32 v248, v248, v249\n"
      "v_cvt_pk_bf16_f32 v249, v250, v251\n"
      "ds_write_b64 v89, v[248:249] offset:192\n"
      "v_pk_mul_f32 v[252:253], v[252:253], v[18:19] op_sel_hi:[1,0]\n"
      "v_pk_mul_f32 v[254:255], v[254:255], v[18:19] op_sel_hi:[1,0]\n"
      "v_cvt_pk_bf16_f32 v252, v252, v253\n"
      "v_cvt_pk_bf16_f32 v253, v254, v255\n"
      "ds_write_b64 v89, v[252:253] offset:224\n"
      "ds_read_b128 v[240:243], v90 offset:0\n"
      "ds_read_b128 v[244:247], v90 offset:1088\n"
      "ds_read_b128 v[248:251], v90 offset:2176\n"
      "ds_read_b128 v[252:255], v90 offset:3264\n"
      "s_mov_b32 s39, 20480\n"
      "s_waitcnt lgkmcnt(3)\n"
      "buffer_store_dwordx4 v[240:243], v88, s[28:31], s39 offen sc1 nt\n"
      "s_waitcnt lgkmcnt(2)\n"
      "buffer_store_dwordx4 v[244:247], v88, s[28:31], s39 offen offset:1024 sc1 nt\n"
      "s_waitcnt lgkmcnt(1)\n"
      "buffer_store_dwordx4 v[248:251], v88, s[28:31], s39 offen offset:2048 sc1 nt\n"
      "s_waitcnt lgkmcnt(0)\n"
      "buffer_store_dwordx4 v[252:255], v88, s[28:31], s39 offen offset:3072 sc1 nt\n"
      "s_mov_b64 %0, s[44:45]\n"
      : "=s"(flag)
      : "s"(ka), "s"(head), "v"(tid), "s"(ldsb)
      : "memory", "vcc", "scc", "v1", "v2", "v3", "v4", "v5", "v6", "v7", "v8", "v9", "v10", "v11", "v12", "v13", "v14", "v15", "v16",
        "v17", "v18", "v19", "v20", "v21", "v22", "v23", "v24", "v25", "v26", "v27", "v28", "v29", "v30", "v31", "v32",
        "v33", "v34", "v35", "v36", "v37", "v38", "v39", "v40", "v41", "v42", "v43", "v44", "v45", "v46", "v47", "v48",
        "v49", "v50", "v51", "v52", "v53", "v54", "v55", "v56", "v57", "v58", "v59", "v60", "v61", "v62", "v63", "v64",
        "v65", "v66", "v67", "v68", "v69", "v70", "v71", "v72", "v73", "v74", "v75", "v76", "v77", "v78", "v79", "v80",
        "v81", "v82", "v83", "v84", "v85", "v86", "v87", "v88", "v89", "v90", "v91", "v92", "v93", "v94", "v95", "v96",
        "v97", "v98", "v99", "v100", "v101", "v102", "v103", "v104", "v105", "v106", "v107", "v108", "v109", "v110", "v111", "v112",
        "v113", "v114", "v115", "v116", "v117", "v118", "v119", "v120", "v121", "v122", "v123", "v124", "v125", "v126", "v127", "v128",
        "v129", "v130", "v131", "v132", "v133", "v134", "v135", "v136", "v137", "v138", "v139", "v140", "v141", "v142", "v143", "v144",
        "v145", "v146", "v147", "v148", "v149", "v150", "v151", "v152", "v153", "v154", "v155", "v156", "v157", "v158", "v159", "v160",
        "v161", "v162", "v163", "v164", "v165", "v166", "v167", "v168", "v169", "v170", "v171", "v172", "v173", "v174", "v175", "v176",
        "v177", "v178", "v179", "v180", "v181", "v182", "v183", "v184", "v185", "v186", "v187", "v188", "v189", "v190", "v191", "v192",
        "v193", "v194", "v195", "v196", "v197", "v198", "v199", "v200", "v201", "v202", "v203", "v204", "v205", "v206", "v207", "v208",
        "v209", "v210", "v211", "v212", "v213", "v214", "v215", "v216", "v217", "v218", "v219", "v220", "v221", "v222", "v223", "v224",
        "v225", "v226", "v227", "v228", "v229", "v230", "v231", "v232", "v233", "v234", "v235", "v236", "v237", "v238", "v239", "v240",
        "v241", "v242", "v243", "v244", "v245", "v246", "v247", "v248", "v249", "v250", "v251", "v252", "v253", "v254", "v255", "a0",
        "a1", "a2", "a3", "a4", "a5", "a6", "a7", "a8", "a9", "a10", "a11", "a12", "a13", "a14", "a15", "a16",
        "a17", "a18", "a19", "a20", "a21", "a22", "a23", "a24", "a25", "a26", "a27", "a28", "a29", "a30", "a31", "a32",
        "a33", "a34", "a35", "a36", "a37", "a38", "a39", "a40", "a41", "a42", "a43", "a44", "a45", "a46", "a47", "a48",
        "a49", "a50", "a51", "a52", "a53", "a54", "a55", "a56", "a57", "a58", "a59", "a60", "a61", "a62", "a63", "a64",
        "a65", "a66", "a67", "a68", "a69", "a70", "a71", "a72", "a73", "a74", "a75", "a76", "a77", "a78", "a79", "a80",
        "a81", "a82", "a83", "a84", "a85", "a86", "a87", "a88", "a89", "a90", "a91", "a92", "a93", "a94", "a95", "a96",
        "a97", "a98", "a99", "a100", "a101", "a102", "a103", "a104", "a105", "a106", "a107", "a108", "a109", "a110", "a111", "a112",
        "a113", "a114", "a115", "a116", "a117", "a118", "a119", "a120", "a121", "a122", "a123", "a124", "a125", "a126", "a127", "a128",
        "a129", "a130", "a131", "a132", "a133", "a134", "a135", "a136", "a137", "a138", "a139", "a140", "a141", "a142", "a143", "a144",
        "a145", "a146", "a147", "a148", "a149", "a150", "a151", "a152", "a153", "a154", "a155", "a156", "a157", "a158", "a159", "a160",
        "a161", "a162", "a163", "a164", "a165", "a166", "a167", "a168", "a169", "a170", "a171", "a172", "a173", "a174", "a175", "a176",
        "a177", "a178", "a179", "a180", "a181", "a182", "a183", "a184", "a185", "a186", "a187", "a188", "a189", "a190", "a191", "a192",
        "a193", "a194", "a195", "a196", "a197", "a198", "a199", "a200", "a201", "a202", "a203", "a204", "a205", "a206", "a207", "a208",
        "a209", "a210", "a211", "a212", "a213", "a214", "a215", "a216", "a217", "a218", "a219", "a220", "a221", "a222", "a223", "a224",
        "a225", "a226", "a227", "a228", "a229", "a230", "a231", "a232", "a233", "a234", "a235", "a236", "a237", "a238", "a239", "a240",
        "a241", "a242", "a243", "a244", "a245", "a246", "a247", "a248", "a249", "a250", "a251", "a252", "a253", "a254", "a255", "s16",
        "s17", "s18", "s19", "s20", "s21", "s22", "s23", "s24", "s25", "s26", "s27", "s28", "s29", "s30", "s31", "s36",
        "s37", "s38", "s39", "s40", "s41", "s42", "s43", "s44", "s45", "s46", "s47", "s48", "s49", "s50", "s51", "s52",
        "s53", "s54", "s55");
  if (flag != 0ull) {
    asm volatile("s_waitcnt vmcnt(0)" ::: "memory");
    const size_t hb = (size_t)head * S_LEN * HD;
    const unsigned long long* kp = (const unsigned long long*)ka;
    fallback_rows((const u16*)kp[0] + hb, (const u16*)kp[1] + hb, (const u16*)kp[2] + hb, (__hip_bfloat16*)kp[3] + hb, (int)(tid >> 6) * 96, (int)(tid & 63));
  }
}

extern "C" void kernel_launch(void* const* d_in, const int* in_sizes, int n_in, void* d_out, int out_size, void* d_ws, size_t ws_size,
                              hipStream_t stream) {
  hipLaunchKernelGGL(mha_fwd_kernel, dim3(256), dim3(256), 0, stream, (const u16*)d_in[0], (const u16*)d_in[1], (const u16*)d_in[2],
                     (__hip_bfloat16*)d_out);
}
```
